```python
import torch

def _softmax(x, axis=-1):
    return torch.softmax(x, dim=axis)
def _gelu(x):
    return torch.nn.functional.gelu(x, approximate="tanh")
def _silu(x):
    return torch.nn.functional.silu(x)
def _sigmoid(x):
    return torch.sigmoid(x)
def _layernorm(x, axis=-1, eps=1e-5):
    m = x.mean(dim=axis, keepdim=True)
    v = x.var(dim=axis, keepdim=True, unbiased=False)
    return (x - m) / torch.sqrt(v + eps)
def _scatter_add(tgt, idx, src, axis=0):
    return tgt.clone().index_add_(axis, idx, src)
def _segment_sum(src, seg, n):
    out = torch.zeros((n,) + src.shape[1:], dtype=src.dtype)
    return out.index_add_(0, seg, src)
def _pad(x, pw):
    flat = tuple(v for pair in reversed(pw) for v in pair)
    return torch.nn.functional.pad(x, flat)

kernel_name = 'gated_mix__sliding_block_cached__glu_gated_swiglu__decode'

def setup_inputs():

    _w = (lambda a: (lambda t: t.to(torch.bfloat16) if t.is_floating_point() else t)(torch.as_tensor(a)))
    def _A_setup():
        B, Sq, D = 8, 1, 512
        Hq, Hkv, Sk, Dh = 32, 32, 4096, 128
        assert Hq % Hkv == 0, (Hq, Hkv)
        return {
            "x": _w((lambda *s: torch.randn(*s, dtype=torch.bfloat16) * 0.05)(B, Sq, D)),
            "k_cache": _w((lambda *s: torch.randn(*s, dtype=torch.bfloat16) * 3.2)(B, Hkv, Sk, Dh)),
            "v_cache": _w((lambda *s: torch.randn(*s, dtype=torch.bfloat16) * 0.05)(B, Hkv, Sk, Dh)),
            "wq": _w((lambda *s: torch.randn(*s, dtype=torch.bfloat16) * 3.2)(D, Hq * Dh) * (D ** -0.5)),
            "wo": _w((lambda *s: torch.randn(*s, dtype=torch.bfloat16) * 0.05)(Hq * Dh, D) * ((Hq * Dh) ** -0.5)),
        }
    _A_in = _A_setup()

    def _B_setup():
        B, S, D, F = 8, 1, 512, 1376
        return {
            "x": _w((lambda *s: torch.randn(*s, dtype=torch.bfloat16) * 0.05)(B, S, D)),
            "w1": _w((lambda *s: torch.randn(*s, dtype=torch.bfloat16) * 0.2)(D, F) * (2.0 / D) ** 0.5),
            "w3": _w((lambda *s: torch.randn(*s, dtype=torch.bfloat16) * 0.2)(D, F) * (2.0 / D) ** 0.5),
            "w2": _w((lambda *s: torch.randn(*s, dtype=torch.bfloat16) * 0.2)(F, D) * (2.0 / F) ** 0.5),
        }
    _B_in = _B_setup()

    return {
        "A_x": _A_in["x"],
        "A_k_cache": _A_in["k_cache"],
        "A_v_cache": _A_in["v_cache"],
        "A_wq": _A_in["wq"],
        "A_wo": _A_in["wo"],
        "B_x": _B_in["x"],
        "B_w1": _B_in["w1"],
        "B_w3": _B_in["w3"],
        "B_w2": _B_in["w2"],
        "gate_w": _w((lambda *s: torch.randn(*s, dtype=torch.bfloat16) * 0.05)(512) * 16.0),
    }

def reference(A_x, A_k_cache, A_v_cache, A_wq, A_wo, B_x, B_w1, B_w3, B_w2, gate_w):
    def _A_ref(x, k_cache, v_cache, wq, wo):
        B, Sq, D = x.shape
        Hq, Dh = 32, 128
        Hkv, Sk = k_cache.shape[1], k_cache.shape[2]
        W = min(4096, max(256, Sk // 4))
        rep = Hq // Hkv
        q = (lambda x, *d: x.permute(*d))((x @ wq).reshape(B, Sq, Hq, Dh), 0, 2, 1, 3)
        k = (lambda x, n, axis: torch.repeat_interleave(x, n, dim=axis))(k_cache[:, :, Sk - W:, :], rep, axis=1)
        v = (lambda x, n, axis: torch.repeat_interleave(x, n, dim=axis))(v_cache[:, :, Sk - W:, :], rep, axis=1)
        scores = torch.einsum("bhqd,bhkd->bhqk", q, k) * (Dh ** -0.5)
        p = _softmax(scores, axis=-1)
        o = torch.einsum("bhqk,bhkd->bhqd", p, v)
        return (lambda x, *d: x.permute(*d))(o, 0, 2, 1, 3).reshape(B, Sq, Hq * Dh) @ wo

    def _B_ref(x, w1, w3, w2):
        gate = torch.einsum("bsd,df->bsf", x, w1)
        up = torch.einsum("bsd,df->bsf", x, w3)
        h = _silu(gate) * up
        out = torch.einsum("bsf,fd->bsd", h, w2)
        return out

    x = A_x
    g = _sigmoid((x * gate_w).sum(-1, keepdims=True))
    a = _A_ref(x, A_k_cache, A_v_cache, A_wq, A_wo)
    b = _B_ref(x, B_w1, B_w3, B_w2)
    return g * a + (1 - g) * b
```

```cpp
#include <hip/hip_runtime.h>
#include <hip/hip_bf16.h>
#include <stdint.h>

typedef unsigned v4u __attribute__((ext_vector_type(4)));
typedef unsigned v2u __attribute__((ext_vector_type(2)));
typedef float f32x4 __attribute__((ext_vector_type(4)));
typedef float f32x2 __attribute__((ext_vector_type(2)));
typedef __bf16 bf16x8 __attribute__((ext_vector_type(8)));
typedef __bf16 bf16x2 __attribute__((ext_vector_type(2)));
#define GAS __attribute__((address_space(1)))
#define DEVI __device__ __forceinline__

#define WIN 40
#define AUXKV 2
#define AUXW 0
#define KEYMAP 1

constexpr int NB = 8, DM = 512, NH = 32, SK = 4096, DH = 128, WK = 1024, FF = 1376;
constexpr int NCH = FF / 8;
constexpr int NWAVE = 8, NTHR = NWAVE * 64;
constexpr int RSRC_W3 = 0x00020000;

template <int N> struct IC { static constexpr int value = N; };
template <int LO, int HI, class Fn> DEVI void static_for(Fn&& f) {
  if constexpr (LO < HI) { f(IC<LO>{}); static_for<LO + 1, HI>(f); }
}

DEVI float dot2(unsigned a, unsigned b, float c) {
  return __builtin_amdgcn_fdot2_f32_bf16(__builtin_bit_cast(bf16x2, a), __builtin_bit_cast(bf16x2, b), c, false);
}
DEVI unsigned cvtpk(float lo, float hi) {
  bf16x2 r = {(__bf16)lo, (__bf16)hi};
  return __builtin_bit_cast(unsigned, r);
}
template <int CTRL> DEVI float dppf(float x) {
  return __builtin_bit_cast(float, __builtin_amdgcn_mov_dpp(__builtin_bit_cast(int, x), CTRL, 0xf, 0xf, true));
}
template <int CTRL> DEVI unsigned dppu(unsigned x) {
  return (unsigned)__builtin_amdgcn_mov_dpp((int)x, CTRL, 0xf, 0xf, true);
}
constexpr int XOR1 = 0xB1, XOR2 = 0x4E, XOR7 = 0x141, XOR8 = 0x128, ROR4 = 0x124, BCAST = 0x150;
DEVI float xrow_max(float x) {
  auto s = __builtin_amdgcn_permlane16_swap(__float_as_uint(x), __float_as_uint(x), false, false);
  x = fmaxf(__uint_as_float(s[0]), __uint_as_float(s[1]));
  auto t = __builtin_amdgcn_permlane32_swap(__float_as_uint(x), __float_as_uint(x), false, false);
  return fmaxf(__uint_as_float(t[0]), __uint_as_float(t[1]));
}
DEVI float xrow_sum(float x) {
  auto s = __builtin_amdgcn_permlane16_swap(__float_as_uint(x), __float_as_uint(x), false, false);
  x = __uint_as_float(s[0]) + __uint_as_float(s[1]);
  auto t = __builtin_amdgcn_permlane32_swap(__float_as_uint(x), __float_as_uint(x), false, false);
  return __uint_as_float(t[0]) + __uint_as_float(t[1]);
}
DEVI float wave_max(float x) {
  x = fmaxf(x, dppf<XOR1>(x)); x = fmaxf(x, dppf<XOR2>(x)); x = fmaxf(x, dppf<XOR7>(x)); x = fmaxf(x, dppf<XOR8>(x));
  return xrow_max(x);
}
DEVI float wave_sum(float x) {
  x += dppf<XOR1>(x); x += dppf<XOR2>(x); x += dppf<XOR7>(x); x += dppf<XOR8>(x);
  return xrow_sum(x);
}
template <int T> DEVI f32x4 rowmfma(unsigned word, const v4u& sel, v4u b, f32x4 acc) {
  const unsigned wb = dppu<BCAST + T>(word);
  v4u a = {__builtin_amdgcn_perm(wb, wb, sel[0]), __builtin_amdgcn_perm(wb, wb, sel[1]),
           __builtin_amdgcn_perm(wb, wb, sel[2]), __builtin_amdgcn_perm(wb, wb, sel[3])};
  return __builtin_amdgcn_mfma_f32_16x16x32_bf16(__builtin_bit_cast(bf16x8, a), __builtin_bit_cast(bf16x8, b), acc, 0, 0, 0);
}

struct Ctx {
  __amdgpu_buffer_rsrc_t rq, rk, rv, r1, r3, ro, r2;
  int vq, vkv, v13, vo;
};
constexpr int S_WQ = 0, S_K = 16, S_W13 = 48, S_V0 = 64, S_WO = 80, S_W2 = 96, S_V1 = 102, S_END = 118;
constexpr int KVSTEP = KEYMAP ? 8192 : 1024;
template <int OFF, int AUX> DEVI v4u bld(__amdgpu_buffer_rsrc_t r, int v) {
  return __builtin_amdgcn_raw_buffer_load_b128(r, v + (OFF & 4095), OFF & ~4095, AUX);
}
template <int I> DEVI v4u ld(const Ctx& c) {
  if constexpr (I < S_K) return bld<(I - S_WQ) * 4 * 8192, AUXW>(c.rq, c.vq);
  else if constexpr (I < S_W13) return bld<(I - S_K) * KVSTEP, AUXKV>(c.rk, c.vkv);
  else if constexpr (I < S_W13 + 8) return bld<(I - S_W13) * 8 * FF * 2, AUXW>(c.r1, c.v13);
  else if constexpr (I < S_V0) return bld<(I - S_W13 - 8) * 8 * FF * 2, AUXW>(c.r3, c.v13);
  else if constexpr (I < S_WO) return bld<(I - S_V0) * KVSTEP, AUXKV>(c.rv, c.vkv);
  else if constexpr (I < S_W2) return bld<(I - S_WO) * 8 * 1024, AUXW>(c.ro, c.vo);
  else if constexpr (I < S_V1) return bld<(I - S_W2) * 8 * 1024, AUXW>(c.r2, c.vo);
  else return bld<(I - S_V1 + 16) * KVSTEP, AUXKV>(c.rv, c.vkv);
}

struct Params {
  const unsigned short *x, *k, *v, *wq, *wo, *w1, *w3, *w2, *gw;
  unsigned short* out;
  float* ws;
};

constexpr int L_Q = 0;
constexpr int L_GU = L_Q + 8 * 128;
constexpr int L_ST = L_GU + 8 * 256;
constexpr int L_O = L_ST + 16;
constexpr int L_END = L_O + 8 * 256;

__global__ void __launch_bounds__(NTHR, 2) attn_ffn_kernel(Params p) {
  __shared__ __attribute__((aligned(16))) float lds[L_END];
  const int tid = threadIdx.x, lane = tid & 63, w = __builtin_amdgcn_readfirstlane(tid >> 6);
  const int r = lane >> 4, c = lane & 15;
  const int bid = blockIdx.x, h = bid & 31, b = bid >> 5;
  const int hp = (h & 7) * 4 + (h >> 3);
  const int ch0 = (NCH * hp) >> 5, nch = ((NCH * (hp + 1)) >> 5) - ch0;
  Ctx cx;
  const size_t kvo = ((size_t)(b * NH + h) * SK + (SK - WK)) * DH;
  cx.rk = __builtin_amdgcn_make_buffer_rsrc((void*)(p.k + kvo), 0, WK * DH * 2, RSRC_W3);
  cx.rv = __builtin_amdgcn_make_buffer_rsrc((void*)(p.v + kvo), 0, WK * DH * 2, RSRC_W3);
  cx.rq = __builtin_amdgcn_make_buffer_rsrc((void*)(p.wq), 0, DM * NH * DH * 2, RSRC_W3);
  cx.r1 = __builtin_amdgcn_make_buffer_rsrc((void*)(p.w1), 0, DM * FF * 2, RSRC_W3);
  cx.r3 = __builtin_amdgcn_make_buffer_rsrc((void*)(p.w3), 0, DM * FF * 2, RSRC_W3);
  cx.ro = __builtin_amdgcn_make_buffer_rsrc((void*)(p.wo + (size_t)h * DH * DM), 0, DH * DM * 2, RSRC_W3);
  cx.r2 = __builtin_amdgcn_make_buffer_rsrc((void*)(p.w2 + (size_t)ch0 * 8 * DM), 0, nch * 8 * DM * 2, RSRC_W3);
  cx.vq = (64 * w + r) * 8192 + h * 256 + c * 16;
  cx.vkv = (KEYMAP ? w * 1024 : w * 32768) + lane * 16;
  cx.v13 = ((c & 7) < nch) ? (64 * w + 2 * r + (c >> 3)) * (FF * 2) + (ch0 + (c & 7)) * 16 : 0x40000000;
  cx.vo = (2 * r + (c >> 3)) * 1024 + w * 128 + (c & 7) * 16;

  const GAS unsigned short* xb = (const GAS unsigned short*)p.x + b * DM;
  const unsigned xq = xb[64 * w + 4 * c + r];
  const unsigned xp = *(const GAS unsigned*)(xb + 64 * w + 8 * (c & 7) + 2 * r);
  const v4u xg = *(const GAS v4u*)(xb + 8 * lane);
  const v4u gw = *(const GAS v4u*)((const GAS unsigned short*)p.gw + 8 * lane);
  __builtin_amdgcn_sched_barrier(0);

  v4u a[WIN];
  static_for<0, WIN>([&](auto I) { a[I.value] = ld<I.value>(cx); });
  __builtin_amdgcn_sched_barrier(0);

  v4u sel;
  {
    const unsigned hb = (c >> 3) ? 0x0302u : 0x0100u;
    const unsigned s = (c & 1) ? ((hb << 16) | 0x0c0cu) : (0x0c0c0000u | hb);
#pragma unroll
    for (int d = 0; d < 4; ++d) sel[d] = (((c & 7) >> 1) == d) ? s : 0x0c0c0c0cu;
  }
#define STEP(I, cur) const v4u cur = a[(I) % WIN]; if constexpr ((I) + WIN < S_END) a[(I) % WIN] = ld<(I) + WIN>(cx);

  f32x4 acc = {0.f, 0.f, 0.f, 0.f};
  static_for<0, 16>([&](auto T) {
    STEP(S_WQ + T.value, cur)
    acc = rowmfma<T.value>(xq, sel, cur, acc);
    __builtin_amdgcn_sched_barrier(0);
  });
  if (r < 2) *(f32x4*)&lds[L_Q + w * 128 + 8 * c + 4 * r] = acc;
  __syncthreads();
  v4u qv;
  {
    f32x4 q0 = {0.f, 0.f, 0.f, 0.f}, q1 = q0;
#pragma unroll
    for (int ww = 0; ww < 8; ++ww) {
      q0 += *(const f32x4*)&lds[L_Q + ww * 128 + 8 * c];
      q1 += *(const f32x4*)&lds[L_Q + ww * 128 + 8 * c + 4];
    }
    qv = v4u{cvtpk(q0[0], q0[1]), cvtpk(q0[2], q0[3]), cvtpk(q1[0], q1[1]), cvtpk(q1[2], q1[3])};
  }
  __builtin_amdgcn_sched_barrier(0);
  float sc[2];
  static_for<0, 2>([&](auto U) {
    float x[16];
    static_for<0, 16>([&](auto T) {
      STEP(S_K + 16 * U.value + T.value, cur)
      float d = dot2(cur[0], qv[0], 0.f);
      d = dot2(cur[1], qv[1], d); d = dot2(cur[2], qv[2], d); d = dot2(cur[3], qv[3], d);
      x[T.value] = d;
      __builtin_amdgcn_sched_barrier(0);
    });
#define TR_STEP(HALF, CTRL) _Pragma("unroll") for (int i = 0; i < (HALF); ++i) { \
      const float lo_ = x[i], hi_ = x[i + (HALF)]; const float t1_ = lo_ + dppf<CTRL>(lo_), t2_ = hi_ + dppf<CTRL>(hi_); \
      x[i] = (c & (HALF)) ? t2_ : t1_; }
    TR_STEP(8, XOR8) TR_STEP(4, XOR7) TR_STEP(2, XOR2) TR_STEP(1, XOR1)
#undef TR_STEP
    sc[U.value] = x[0] * (0.08838834764831845f * 1.4426950408889634f);
    __builtin_amdgcn_sched_barrier(0);
  });
  const float mw = wave_max(fmaxf(sc[0], sc[1]));
  float e0 = __builtin_amdgcn_exp2f(sc[0] - mw), e1 = __builtin_amdgcn_exp2f(sc[1] - mw);
  const float lw = wave_sum(e0 + e1);
  if (lane == 0) *(f32x2*)&lds[L_ST + 2 * w] = f32x2{mw, lw};
  {
    f32x4 ag = {0.f, 0.f, 0.f, 0.f}, au = ag;
    static_for<0, 8>([&](auto T) {
      STEP(S_W13 + T.value, cur)
      ag = rowmfma<T.value>(xp, sel, cur, ag);
      __builtin_amdgcn_sched_barrier(0);
    });
    static_for<0, 8>([&](auto T) {
      STEP(S_W13 + 8 + T.value, cur)
      au = rowmfma<T.value>(xp, sel, cur, au);
      __builtin_amdgcn_sched_barrier(0);
    });
    if ((r >> 1) == (c >> 3)) {
      *(f32x4*)&lds[L_GU + w * 256 + (r >> 1) * 128 + 8 * (c & 7) + 4 * (r & 1)] = ag;
      *(f32x4*)&lds[L_GU + w * 256 + (r >> 1) * 128 + 64 + 8 * (c & 7) + 4 * (r & 1)] = au;
    }
  }
  __syncthreads();
  unsigned pw0, pw1, hw;
  {
    float M = -INFINITY;
    f32x2 st[8];
#pragma unroll
    for (int ww = 0; ww < 8; ++ww) { st[ww] = *(const f32x2*)&lds[L_ST + 2 * ww]; M = fmaxf(M, st[ww][0]); }
    float L = 0.f;
#pragma unroll
    for (int ww = 0; ww < 8; ++ww) L += st[ww][1] * __builtin_amdgcn_exp2f(st[ww][0] - M);
    const float f = __builtin_amdgcn_exp2f(mw - M) / L;
    e0 *= f; e1 *= f;
    const float h0 = __uint_as_float(cvtpk(e0, 0.f) << 16), h1 = __uint_as_float(cvtpk(e1, 0.f) << 16);
    pw0 = cvtpk(e0, e0 - h0); pw1 = cvtpk(e1, e1 - h1);
    f32x2 s = {0.f, 0.f};
#pragma unroll
    for (int i = 0; i < 16; ++i) s += *(const f32x2*)&lds[L_GU + i * 128 + (c >> 3) * 64 + 8 * (c & 7) + 2 * r];
    const float o0 = dppf<XOR8>(s[0]), o1 = dppf<XOR8>(s[1]);
    const float a0 = s[0] / (1.f + __expf(-s[0])) * o0, a1 = s[1] / (1.f + __expf(-s[1])) * o1;
    hw = (c < nch) ? cvtpk(a0, a1) : 0u;
  }
  float g;
  {
    float d = dot2(xg[0], gw[0], 0.f);
    d = dot2(xg[1], gw[1], d); d = dot2(xg[2], gw[2], d); d = dot2(xg[3], gw[3], d);
    d = wave_sum(d);
    g = 1.f / (1.f + __expf(-d));
  }
  __builtin_amdgcn_sched_barrier(0);
  f32x4 ao = {0.f, 0.f, 0.f, 0.f};
  static_for<0, 16>([&](auto T) {
    STEP(S_V0 + T.value, cur)
    ao = rowmfma<T.value>(pw0, sel, cur, ao);
    __builtin_amdgcn_sched_barrier(0);
  });
  v4u wo[16];
  static_for<0, 16>([&](auto T) {
    STEP(S_WO + T.value, cur)
    wo[T.value] = cur;
  });
  __builtin_amdgcn_sched_barrier(0);
  f32x4 af = {0.f, 0.f, 0.f, 0.f};
  static_for<0, 6>([&](auto T) {
    STEP(S_W2 + T.value, cur)
    af = rowmfma<T.value>(hw, sel, cur, af);
    __builtin_amdgcn_sched_barrier(0);
  });
  static_for<0, 16>([&](auto T) {
    STEP(S_V1 + T.value, cur)
    ao = rowmfma<T.value>(pw1, sel, cur, ao);
    __builtin_amdgcn_sched_barrier(0);
  });
  *(f32x4*)&lds[L_O + w * 256 + (r >> 1) * 128 + 8 * c + 4 * (r & 1)] = ao;
  __syncthreads();
  unsigned ow;
  {
    f32x2 s = {0.f, 0.f};
#pragma unroll
    for (int i = 0; i < 16; ++i) s += *(const f32x2*)&lds[L_O + i * 128 + 8 * c + 2 * r];
    ow = cvtpk(s[0], s[1]);
  }
  f32x4 ap0 = {0.f, 0.f, 0.f, 0.f}, ap1 = ap0;
  static_for<0, 8>([&](auto T) {
    ap0 = rowmfma<2 * T.value>(ow, sel, wo[2 * T.value], ap0);
    ap1 = rowmfma<2 * T.value + 1>(ow, sel, wo[2 * T.value + 1], ap1);
  });
  f32x4 res = (ap0 + ap1) * g + af * (1.f - g);
#pragma unroll
  for (int i = 0; i < 4; ++i) {
    const float y = dppf<XOR8>(res[i]);
    auto t = __builtin_amdgcn_permlane32_swap(__float_as_uint(res[i]), __float_as_uint(y), false, false);
    (void)t;
    auto u = __builtin_amdgcn_permlane32_swap(__float_as_uint(y), __float_as_uint(y), false, false);
    res[i] = res[i] + __uint_as_float(u[1]);
  }
  if (r < 2 && c < 8) *(f32x4*)&p.ws[(size_t)(b * NH + h) * DM + 64 * w + 8 * c + 4 * r] = res;
}

__global__ void __launch_bounds__(64) reduce_kernel(const float* ws, unsigned short* out) {
  const int i = blockIdx.x * 64 + threadIdx.x;
  const int b = i >> 9, n = i & 511;
  float s = 0.f;
#pragma unroll
  for (int h = 0; h < NH; ++h) s += ws[(size_t)(b * NH + h) * DM + n];
  out[i] = (unsigned short)(cvtpk(s, 0.f) & 0xffffu);
}

extern "C" void kernel_launch(void* const* d_in, const int* in_sizes, int n_in, void* d_out, int out_size, void* d_ws,
                              size_t ws_size, hipStream_t stream) {
  Params p;
  p.x = (const unsigned short*)d_in[0];
  p.k = (const unsigned short*)d_in[1];
  p.v = (const unsigned short*)d_in[2];
  p.wq = (const unsigned short*)d_in[3];
  p.wo = (const unsigned short*)d_in[4];
  p.w1 = (const unsigned short*)d_in[6];
  p.w3 = (const unsigned short*)d_in[7];
  p.w2 = (const unsigned short*)d_in[8];
  p.gw = (const unsigned short*)d_in[9];
  p.out = (unsigned short*)d_out;
  p.ws = (float*)d_ws;
  hipLaunchKernelGGL(attn_ffn_kernel, dim3(NB * NH), dim3(NTHR), 0, stream, p);
  hipLaunchKernelGGL(reduce_kernel, dim3(NB * DM / 64), dim3(64), 0, stream, (const float*)d_ws, (unsigned short*)d_out);
}
```

```cpp
#include <hip/hip_runtime.h>
#include <hip/hip_bf16.h>
#include <stdint.h>

typedef unsigned v4u __attribute__((ext_vector_type(4)));
typedef float f32x4 __attribute__((ext_vector_type(4)));
typedef float f32x2 __attribute__((ext_vector_type(2)));
typedef __bf16 bf16x8 __attribute__((ext_vector_type(8)));
typedef __bf16 bf16x2 __attribute__((ext_vector_type(2)));
#define GAS __attribute__((address_space(1)))
#define DEVI __device__ __forceinline__

#define DEPTH 5
#define AUXKV 2
#define AUXW 0
#define STAG 0
#define GAP 8
#define NSTAMP 0
#define STAMP(i)

constexpr int NB = 8, DM = 512, NH = 32, SK = 4096, DH = 128, WK = 1024, FF = 1376;
constexpr int NCH = FF / 8;
constexpr int NWAVE = 8, NTHR = NWAVE * 64;
constexpr int RSRC_W3 = 0x00020000;

template <int N> struct IC { static constexpr int value = N; };
template <int LO, int HI, class Fn> DEVI void static_for(Fn&& f) {
  if constexpr (LO < HI) { f(IC<LO>{}); static_for<LO + 1, HI>(f); }
}
DEVI float dot2(unsigned a, unsigned b, float c) {
  return __builtin_amdgcn_fdot2_f32_bf16(__builtin_bit_cast(bf16x2, a), __builtin_bit_cast(bf16x2, b), c, false);
}
DEVI unsigned cvtpk(float lo, float hi) {
  bf16x2 r = {(__bf16)lo, (__bf16)hi};
  return __builtin_bit_cast(unsigned, r);
}
template <int CTRL> DEVI float dppf(float x) {
  return __builtin_bit_cast(float, __builtin_amdgcn_mov_dpp(__builtin_bit_cast(int, x), CTRL, 0xf, 0xf, true));
}
template <int CTRL> DEVI unsigned dppu(unsigned x) { return (unsigned)__builtin_amdgcn_mov_dpp((int)x, CTRL, 0xf, 0xf, true); }
constexpr int XOR1 = 0xB1, XOR2 = 0x4E, XOR7 = 0x141, XOR8 = 0x128, BCAST = 0x150;
DEVI float xrow_max(float x) {
  auto s = __builtin_amdgcn_permlane16_swap(__float_as_uint(x), __float_as_uint(x), false, false);
  x = fmaxf(__uint_as_float(s[0]), __uint_as_float(s[1]));
  auto t = __builtin_amdgcn_permlane32_swap(__float_as_uint(x), __float_as_uint(x), false, false);
  return fmaxf(__uint_as_float(t[0]), __uint_as_float(t[1]));
}
DEVI float xrow_sum(float x) {
  auto s = __builtin_amdgcn_permlane16_swap(__float_as_uint(x), __float_as_uint(x), false, false);
  x = __uint_as_float(s[0]) + __uint_as_float(s[1]);
  auto t = __builtin_amdgcn_permlane32_swap(__float_as_uint(x), __float_as_uint(x), false, false);
  return __uint_as_float(t[0]) + __uint_as_float(t[1]);
}
DEVI float wave_max(float x) {
  x = fmaxf(x, dppf<XOR1>(x)); x = fmaxf(x, dppf<XOR2>(x)); x = fmaxf(x, dppf<XOR7>(x)); x = fmaxf(x, dppf<XOR8>(x));
  return xrow_max(x);
}
DEVI float wave_sum(float x) {
  x += dppf<XOR1>(x); x += dppf<XOR2>(x); x += dppf<XOR7>(x); x += dppf<XOR8>(x);
  return xrow_sum(x);
}
DEVI f32x4 mfma(v4u a, v4u b, f32x4 acc) {
  return __builtin_amdgcn_mfma_f32_16x16x32_bf16(__builtin_bit_cast(bf16x8, a), __builtin_bit_cast(bf16x8, b), acc, 0, 0, 0);
}
template <int T> DEVI f32x4 pairmfma(unsigned word, const v4u& sel, v4u b, f32x4 acc) {
  const unsigned wb = dppu<BCAST + T>(word);
  v4u a = {__builtin_amdgcn_perm(wb, wb, sel[0]), __builtin_amdgcn_perm(wb, wb, sel[1]),
           __builtin_amdgcn_perm(wb, wb, sel[2]), __builtin_amdgcn_perm(wb, wb, sel[3])};
  return mfma(a, b, acc);
}
template <int T> DEVI f32x4 rowmfma(unsigned word, const v4u& msk, v4u b, f32x4 acc) {
  v4u a = {dppu<BCAST + T>(word) & msk[0], dppu<BCAST + T>(word) & msk[1], dppu<BCAST + T>(word) & msk[2], dppu<BCAST + T>(word) & msk[3]};
  return mfma(a, b, acc);
}

enum { G_WQ = 0, G_K = 1, G_W13 = 2, G_V = 3, G_WO = 4, G_W2 = 5 };
constexpr int NLOAD = 16 + 32 + 16 + 32 + 16 + 6;
struct Sched { int seg[NLOAD], idx[NLOAD]; int q_done, st_done; };
constexpr Sched make_sched(int j) {
  Sched s{}; int n = 0;
  const int pq = STAG ? 4 + 4 * j : 6, p13 = STAG ? (j == 3 ? 32 : 18 + 5 * j) : 26, po = STAG ? 40 + 5 * j : 48;
  auto put = [&](int g, int i) { s.seg[n] = g; s.idx[n] = i; ++n; };
  put(G_WQ, 0); put(G_WQ, 1);
  for (int u = 0; u <= 64; ++u) {
    if (u == pq) { for (int i = 2; i < 16; ++i) put(G_WQ, i); s.q_done = n - 1; }
    if (u == p13 - GAP) { put(G_W13, 0); put(G_W13, 8); }
    if (u == p13) { for (int i = 1; i < 8; ++i) put(G_W13, i); for (int i = 9; i < 16; ++i) put(G_W13, i); }
    if (u == 32) s.st_done = n - 1;
    if (u == po - GAP) { put(G_WO, 0); put(G_WO, 1); put(G_W2, 0); }
    if (u == po) { for (int i = 1; i < 6; ++i) put(G_W2, i); for (int i = 2; i < 16; ++i) put(G_WO, i); }
    if (u < 32) put(G_K, u); else if (u < 64) put(G_V, u - 32);
  }
  return s;
}

struct Params {
  const unsigned short *x, *k, *v, *wq, *wo, *w1, *w3, *w2, *gw;
  unsigned short* out;
  float* ws;
};

constexpr int L_Q = 0;
constexpr int L_GU = L_Q + 8 * 128;
constexpr int L_ST = L_GU + 8 * 256;
constexpr int L_O = L_ST + 16;
constexpr int L_END = L_O + 8 * 128;

template <int J> DEVI void body(const Params& p, float* lds) {
  STAMP(0);
  constexpr Sched S = make_sched(J);
  const int tid = threadIdx.x, lane = tid & 63, w = __builtin_amdgcn_readfirstlane(tid >> 6);
  const int r = lane >> 4, c = lane & 15;
  const int bid = blockIdx.x, h = bid & 31, b = bid >> 5;
  const int hp = (h & 7) * 4 + (h >> 3);
  const int ch0 = (NCH * hp) >> 5, nch = ((NCH * (hp + 1)) >> 5) - ch0;
  const size_t kvo = ((size_t)(b * NH + h) * SK + (SK - WK)) * DH;
  const __amdgpu_buffer_rsrc_t rk = __builtin_amdgcn_make_buffer_rsrc((void*)(p.k + kvo), 0, WK * DH * 2, RSRC_W3);
  const __amdgpu_buffer_rsrc_t rv = __builtin_amdgcn_make_buffer_rsrc((void*)(p.v + kvo), 0, WK * DH * 2, RSRC_W3);
  const __amdgpu_buffer_rsrc_t rq = __builtin_amdgcn_make_buffer_rsrc((void*)(p.wq), 0, DM * NH * DH * 2, RSRC_W3);
  const __amdgpu_buffer_rsrc_t r1 = __builtin_amdgcn_make_buffer_rsrc((void*)(p.w1), 0, DM * FF * 2, RSRC_W3);
  const __amdgpu_buffer_rsrc_t r3 = __builtin_amdgcn_make_buffer_rsrc((void*)(p.w3), 0, DM * FF * 2, RSRC_W3);
  const __amdgpu_buffer_rsrc_t ro = __builtin_amdgcn_make_buffer_rsrc((void*)(p.wo + (size_t)h * DH * DM), 0, DH * DM * 2, RSRC_W3);
  const __amdgpu_buffer_rsrc_t r2 = __builtin_amdgcn_make_buffer_rsrc((void*)(p.w2 + (size_t)ch0 * 8 * DM), 0, nch * 8 * DM * 2, RSRC_W3);
  const int vq = (64 * w + r) * 8192 + h * 256 + c * 16;
  const int vkv = w * 4096 + lane * 16;
  const int v13 = ((c & 7) < nch) ? (64 * w + 2 * r + (c >> 3)) * (FF * 2) + (ch0 + (c & 7)) * 16 : 0x40000000;
  const int vo = (2 * r + (c >> 3)) * 1024 + w * 128 + (c & 7) * 16;
  const int b6 = b >= 6 ? b - 6 : b;
  const int c16 = (c + 2 * b) & 15;
  const int c8 = (c + b) & 7;
  const int c6 = ((c & 7) + b6) >= 6 ? (c & 7) + b6 - 6 : (c & 7) + b6;

  const GAS unsigned short* xb = (const GAS unsigned short*)p.x + b * DM;
  const unsigned xq0 = xb[64 * w + 4 * c16 + r];
  v4u wq[16], kk[32], w13[16], vv[32], wo[16], w2[6];
  auto issue = [&](auto N) {
    constexpr int n = N.value;
    if constexpr (n < NLOAD) {
      constexpr int g = S.seg[n], i = S.idx[n];
      if constexpr (g == G_WQ) wq[i] = __builtin_amdgcn_raw_buffer_load_b128(rq, vq, ((i + 2 * b) & 15) * 32768, AUXW);
      if constexpr (g == G_K) kk[i] = __builtin_amdgcn_raw_buffer_load_b128(rk, vkv + (i & 3) * 1024, (i >> 2) * 32768, AUXKV);
      if constexpr (g == G_V) vv[i] = __builtin_amdgcn_raw_buffer_load_b128(rv, vkv + (i & 3) * 1024, (i >> 2) * 32768, AUXKV);
      if constexpr (g == G_W13) w13[i] = __builtin_amdgcn_raw_buffer_load_b128(i < 8 ? r1 : r3, v13, ((i + b) & 7) * (8 * FF * 2), AUXW);
      if constexpr (g == G_WO) wo[i] = __builtin_amdgcn_raw_buffer_load_b128(ro, vo, ((i + 2 * b) & 15) * 8192, AUXW);
      if constexpr (g == G_W2) w2[i] = __builtin_amdgcn_raw_buffer_load_b128(r2, vo, (i + b6 >= 6 ? i + b6 - 6 : i + b6) * 8192, AUXW);
    }
    __builtin_amdgcn_sched_barrier(0);
  };
  static_for<0, DEPTH>([&](auto N) { issue(N); });
  const unsigned xp = *(const GAS unsigned*)(xb + 64 * w + 8 * c8 + 2 * r);
  const v4u xg = *(const GAS v4u*)(xb + 8 * lane);
  const v4u gw = *(const GAS v4u*)((const GAS unsigned short*)p.gw + 8 * lane);
  float* wsp = p.ws + (size_t)(b * NH + h) * DM + 64 * w + 8 * c + 4 * r;
  asm volatile("" : "+v"(wsp));
  v4u sel, msk;
  {
    const unsigned hb = (c >> 3) ? 0x0302u : 0x0100u;
    const unsigned s = (c & 1) ? ((hb << 16) | 0x0c0cu) : (0x0c0c0000u | hb);
    const unsigned m = (c & 1) ? 0xffff0000u : 0x0000ffffu;
#pragma unroll
    for (int d = 0; d < 4; ++d) { sel[d] = (((c & 7) >> 1) == d) ? s : 0x0c0c0c0cu; msk[d] = ((c >> 1) == d) ? m : 0u; }
  }
  const unsigned xq = xq0 | (xq0 << 16);
  __builtin_amdgcn_sched_barrier(0);

  f32x4 aq = {0.f, 0.f, 0.f, 0.f}, ag = aq, au = aq, ao = aq, af = aq;
  v4u qv;
  float xs[32], sc[2], mw, e0, e1, g;
  unsigned pw[2], hw;
  auto kdot = [&](auto I) {
    constexpr int i = I.value;
    float d = dot2(kk[i][0], qv[0], 0.f);
    d = dot2(kk[i][1], qv[1], d); d = dot2(kk[i][2], qv[2], d); d = dot2(kk[i][3], qv[3], d);
    xs[i] = d;
    if constexpr ((i & 15) == 15) {
      float* x = xs + (i & 16);
#define TR_STEP(HALF, CTRL) _Pragma("unroll") for (int u = 0; u < (HALF); ++u) { \
      const float lo_ = x[u], hi_ = x[u + (HALF)]; const float t1_ = lo_ + dppf<CTRL>(lo_), t2_ = hi_ + dppf<CTRL>(hi_); \
      x[u] = (c & (HALF)) ? t2_ : t1_; }
      TR_STEP(8, XOR8) TR_STEP(4, XOR7) TR_STEP(2, XOR2) TR_STEP(1, XOR1)
#undef TR_STEP
      sc[i >> 4] = x[0] * (0.08838834764831845f * 1.4426950408889634f);
    }
  };
  static_for<0, NLOAD>([&](auto N) {
    constexpr int n = N.value, gseg = S.seg[n], i = S.idx[n];
    if constexpr (gseg == G_WQ) aq = rowmfma<i>(xq, msk, wq[i], aq);
    if constexpr (gseg == G_K) { if constexpr (n < S.q_done) asm volatile("" :: "v"(kk[i])); else kdot(IC<i>{}); }
    if constexpr (gseg == G_W13) { if constexpr (i < 8) ag = pairmfma<i>(xp, sel, w13[i], ag); else au = pairmfma<i - 8>(xp, sel, w13[i], au); }
    if constexpr (gseg == G_V) ao = rowmfma<(i & 15)>(pw[i >> 4], msk, vv[i], ao);
    if constexpr (gseg == G_WO) asm volatile("" :: "v"(wo[i]));
    if constexpr (gseg == G_W2) { af = pairmfma<i>(hw, sel, w2[i], af); asm volatile("" : "+v"(af)); }
    __builtin_amdgcn_sched_barrier(0);
    issue(IC<n + DEPTH>{});
    if constexpr (n == S.q_done) {
      STAMP(1);
      if (r < 2) *(f32x4*)&lds[L_Q + w * 128 + 8 * c + 4 * r] = aq;
      __syncthreads();
      STAMP(2);
      f32x4 q0 = {0.f, 0.f, 0.f, 0.f}, q1 = q0;
#pragma unroll
      for (int ww = 0; ww < 8; ++ww) {
        q0 += *(const f32x4*)&lds[L_Q + ww * 128 + 8 * c];
        q1 += *(const f32x4*)&lds[L_Q + ww * 128 + 8 * c + 4];
      }
      qv = v4u{cvtpk(q0[0], q0[1]), cvtpk(q0[2], q0[3]), cvtpk(q1[0], q1[1]), cvtpk(q1[2], q1[3])};
      static_for<0, n>([&](auto M) { if constexpr (S.seg[M.value] == G_K) kdot(IC<S.idx[M.value]>{}); });
      float d = dot2(xg[0], gw[0], 0.f);
      d = dot2(xg[1], gw[1], d); d = dot2(xg[2], gw[2], d); d = dot2(xg[3], gw[3], d);
      d = wave_sum(d);
      g = 1.f / (1.f + __expf(-d));
      asm volatile("" : "+v"(g));
      STAMP(3);
      __builtin_amdgcn_sched_barrier(0);
    }
    if constexpr (n == S.st_done) {
      mw = wave_max(fmaxf(sc[0], sc[1]));
      e0 = __builtin_amdgcn_exp2f(sc[0] - mw); e1 = __builtin_amdgcn_exp2f(sc[1] - mw);
      const float lw = wave_sum(e0 + e1);
      if (lane == 0) *(f32x2*)&lds[L_ST + 2 * w] = f32x2{mw, lw};
      if ((r >> 1) == (c >> 3)) {
        *(f32x4*)&lds[L_GU + w * 256 + (r >> 1) * 128 + 8 * (c & 7) + 4 * (r & 1)] = ag;
        *(f32x4*)&lds[L_GU + w * 256 + (r >> 1) * 128 + 64 + 8 * (c & 7) + 4 * (r & 1)] = au;
      }
      STAMP(4);
      __syncthreads();
      float M = -INFINITY;
      f32x2 st[8];
#pragma unroll
      for (int ww = 0; ww < 8; ++ww) { st[ww] = *(const f32x2*)&lds[L_ST + 2 * ww]; M = fmaxf(M, st[ww][0]); }
      float L = 0.f;
#pragma unroll
      for (int ww = 0; ww < 8; ++ww) L += st[ww][1] * __builtin_amdgcn_exp2f(st[ww][0] - M);
      const float f = __builtin_amdgcn_exp2f(mw - M) / L;
      e0 *= f; e1 *= f;
      pw[0] = cvtpk(e0, e0); pw[1] = cvtpk(e1, e1);
      f32x2 s = {0.f, 0.f};
#pragma unroll
      for (int u = 0; u < 16; ++u) s += *(const f32x2*)&lds[L_GU + u * 128 + (c >> 3) * 64 + 8 * c6 + 2 * r];
      const float o0 = dppf<XOR8>(s[0]), o1 = dppf<XOR8>(s[1]);
      const float a0 = s[0] / (1.f + __expf(-s[0])) * o0, a1 = s[1] / (1.f + __expf(-s[1])) * o1;
      hw = (c < 6 && c6 < nch) ? cvtpk(a0, a1) : 0u;
      asm volatile("" : "+v"(hw), "+v"(pw[0]), "+v"(pw[1]));
      STAMP(5);
      __builtin_amdgcn_sched_barrier(0);
    }
  });
  STAMP(6);
  if (r < 2) *(f32x4*)&lds[L_O + w * 128 + 8 * c + 4 * r] = ao;
  af *= (1.f - g);
  __syncthreads();
  STAMP(7);
  unsigned ow;
  {
    f32x2 s0 = {0.f, 0.f}, s1 = s0;
#pragma unroll
    for (int u = 0; u < 8; u += 2) {
      s0 += *(const f32x2*)&lds[L_O + u * 128 + 8 * c16 + 2 * r];
      s1 += *(const f32x2*)&lds[L_O + (u + 1) * 128 + 8 * c16 + 2 * r];
    }
    s0 += s1;
    ow = cvtpk(s0[0], s0[1]);
  }
  f32x4 ap0 = {0.f, 0.f, 0.f, 0.f}, ap1 = ap0;
  static_for<0, 8>([&](auto T) {
    ap0 = pairmfma<2 * T.value>(ow, sel, wo[2 * T.value], ap0);
    ap1 = pairmfma<2 * T.value + 1>(ow, sel, wo[2 * T.value + 1], ap1);
  });
  f32x4 res = (ap0 + ap1) * g + af;
#pragma unroll
  for (int i = 0; i < 4; ++i) {
    const float y = dppf<XOR8>(res[i]);
    auto u = __builtin_amdgcn_permlane32_swap(__float_as_uint(y), __float_as_uint(y), false, false);
    res[i] = res[i] + __uint_as_float(u[1]);
  }
  if (r < 2 && c < 8) *(f32x4*)wsp = res;
}

__global__ void __launch_bounds__(NTHR, 2) attn_ffn_kernel(Params p) {
  __shared__ __attribute__((aligned(16))) float lds[L_END];
  body<0>(p, lds);
}

__global__ void __launch_bounds__(64) reduce_kernel(const float* ws, unsigned short* out) {
  const int i = blockIdx.x * 64 + threadIdx.x;
  const int b = i >> 9, n = i & 511;
  float s = 0.f;
#pragma unroll
  for (int h = 0; h < NH; ++h) s += ws[(size_t)(b * NH + h) * DM + n];
  out[i] = (unsigned short)(cvtpk(s, 0.f) & 0xffffu);
}

extern "C" void kernel_launch(void* const* d_in, const int* in_sizes, int n_in, void* d_out, int out_size, void* d_ws,
                              size_t ws_size, hipStream_t stream) {
  Params p;
  p.x = (const unsigned short*)d_in[0];
  p.k = (const unsigned short*)d_in[1];
  p.v = (const unsigned short*)d_in[2];
  p.wq = (const unsigned short*)d_in[3];
  p.wo = (const unsigned short*)d_in[4];
  p.w1 = (const unsigned short*)d_in[6];
  p.w3 = (const unsigned short*)d_in[7];
  p.w2 = (const unsigned short*)d_in[8];
  p.gw = (const unsigned short*)d_in[9];
  p.out = (unsigned short*)d_out;
  p.ws = (float*)d_ws;
  hipLaunchKernelGGL(attn_ffn_kernel, dim3(NB * NH), dim3(NTHR), 0, stream, p);
  hipLaunchKernelGGL(reduce_kernel, dim3(NB * DM / 64), dim3(64), 0, stream, (const float*)d_ws, (unsigned short*)d_out);
}
```

```cpp
#include <hip/hip_runtime.h>
#include <hip/hip_bf16.h>
#include <stdint.h>

typedef unsigned v4u __attribute__((ext_vector_type(4)));
typedef float f32x4 __attribute__((ext_vector_type(4)));
typedef float f32x2 __attribute__((ext_vector_type(2)));
typedef __bf16 bf16x8 __attribute__((ext_vector_type(8)));
typedef __bf16 bf16x2 __attribute__((ext_vector_type(2)));
#define GAS __attribute__((address_space(1)))
#define LAS __attribute__((address_space(3)))
#define DEVI __device__ __forceinline__

#define DEPTH 8
#define HOLD 40
#define RATE 4
#define AUXKV 2
#define AUXW 0
#define T1 6
#define S1 4
#define T2 1
#define S2 6
#define T3 1
#define S3 6
#define NSTAMP 0
#define STAMP(i)

constexpr int NB = 8, DM = 512, NH = 32, SK = 4096, DH = 128, WK = 1024, FF = 1376;
constexpr int NCH = FF / 8;
constexpr int NTHR = 512;
constexpr int RSRC_W3 = 0x00020000;
constexpr int AUX_SC1 = 16;
constexpr int OOB = 0x40000000;
constexpr int WS_R = 0, WS_DATA = 256 * 128, WS_SND = WS_DATA + 256 * 4096, WS_END = WS_SND + 256 * 512;
constexpr int SPIN_CAP = 1 << 18;

template <int N> struct IC { static constexpr int value = N; };
template <int LO, int HI, class Fn> DEVI void static_for(Fn&& f) {
  if constexpr (LO < HI) { f(IC<LO>{}); static_for<LO + 1, HI>(f); }
}
DEVI float dot2(unsigned a, unsigned b, float c) {
  return __builtin_amdgcn_fdot2_f32_bf16(__builtin_bit_cast(bf16x2, a), __builtin_bit_cast(bf16x2, b), c, false);
}
DEVI unsigned cvtpk(float lo, float hi) {
  bf16x2 r = {(__bf16)lo, (__bf16)hi};
  return __builtin_bit_cast(unsigned, r);
}
template <int CTRL> DEVI float dppf(float x) {
  return __builtin_bit_cast(float, __builtin_amdgcn_mov_dpp(__builtin_bit_cast(int, x), CTRL, 0xf, 0xf, true));
}
template <int CTRL> DEVI unsigned dppu(unsigned x) { return (unsigned)__builtin_amdgcn_mov_dpp((int)x, CTRL, 0xf, 0xf, true); }
constexpr int XOR1 = 0xB1, XOR2 = 0x4E, XOR7 = 0x141, XOR8 = 0x128, BCAST = 0x150;
DEVI float xrow_max(float x) {
  auto s = __builtin_amdgcn_permlane16_swap(__float_as_uint(x), __float_as_uint(x), false, false);
  x = fmaxf(__uint_as_float(s[0]), __uint_as_float(s[1]));
  auto t = __builtin_amdgcn_permlane32_swap(__float_as_uint(x), __float_as_uint(x), false, false);
  return fmaxf(__uint_as_float(t[0]), __uint_as_float(t[1]));
}
DEVI float xrow_sum(float x) {
  auto s = __builtin_amdgcn_permlane16_swap(__float_as_uint(x), __float_as_uint(x), false, false);
  x = __uint_as_float(s[0]) + __uint_as_float(s[1]);
  auto t = __builtin_amdgcn_permlane32_swap(__float_as_uint(x), __float_as_uint(x), false, false);
  return __uint_as_float(t[0]) + __uint_as_float(t[1]);
}
DEVI float wave_max(float x) {
  x = fmaxf(x, dppf<XOR1>(x)); x = fmaxf(x, dppf<XOR2>(x)); x = fmaxf(x, dppf<XOR7>(x)); x = fmaxf(x, dppf<XOR8>(x));
  return xrow_max(x);
}
DEVI float wave_sum(float x) {
  x += dppf<XOR1>(x); x += dppf<XOR2>(x); x += dppf<XOR7>(x); x += dppf<XOR8>(x);
  return xrow_sum(x);
}
DEVI f32x4 mfma(v4u a, v4u b, f32x4 acc) {
  return __builtin_amdgcn_mfma_f32_16x16x32_bf16(__builtin_bit_cast(bf16x8, a), __builtin_bit_cast(bf16x8, b), acc, 0, 0, 0);
}
template <int T> DEVI v4u pair_a(unsigned word, const v4u& sel) {
  const unsigned wb = dppu<BCAST + T>(word);
  return v4u{__builtin_amdgcn_perm(wb, wb, sel[0]), __builtin_amdgcn_perm(wb, wb, sel[1]),
             __builtin_amdgcn_perm(wb, wb, sel[2]), __builtin_amdgcn_perm(wb, wb, sel[3])};
}
template <int T> DEVI f32x4 rowmfma(unsigned word, const v4u& msk, v4u b, f32x4 acc) {
  v4u a = {dppu<BCAST + T>(word) & msk[0], dppu<BCAST + T>(word) & msk[1], dppu<BCAST + T>(word) & msk[2], dppu<BCAST + T>(word) & msk[3]};
  return mfma(a, b, acc);
}
DEVI void touch(const v4u& x) { asm volatile("" :: "v"(x)); }
DEVI void fence() { __builtin_amdgcn_sched_barrier(0); }
DEVI void wg_barrier() { asm volatile("s_waitcnt lgkmcnt(0)\n\ts_barrier" ::: "memory"); }
DEVI void nap(int n) { for (int i = 0; i < n; ++i) __builtin_amdgcn_s_sleep(8); }
DEVI void lds_arrive(LAS float* p, int lane) {
  asm volatile("" ::: "memory");
  if (lane == 0) __hip_atomic_fetch_add((LAS unsigned*)p, 1u, __ATOMIC_RELAXED, __HIP_MEMORY_SCOPE_WORKGROUP);
}
DEVI void lds_wait4(LAS float* p) {
  while (__hip_atomic_load((LAS unsigned*)p, __ATOMIC_RELAXED, __HIP_MEMORY_SCOPE_WORKGROUP) < 4u) __builtin_amdgcn_s_sleep(1);
  asm volatile("" ::: "memory");
}

struct Params {
  const unsigned short *x, *k, *v, *wq, *wo, *w1, *w3, *w2, *gw;
  unsigned short* out;
  unsigned char* ws;
};

constexpr int L_Q = 0;
constexpr int L_GU = L_Q + 4 * 128;
constexpr int L_O = L_GU + 4 * 256;
constexpr int L_ST = L_O + 4 * 128;
constexpr int L_USED = L_ST + 8;
constexpr int L_NEW = L_USED + 32;
constexpr int L_CNT = L_NEW + 32;
constexpr int L_STAMP = L_CNT + 4;
constexpr int L_END = L_STAMP + 2 * 16;

__global__ void __launch_bounds__(NTHR, 2) __attribute__((amdgpu_waves_per_eu(2, 2))) fused_kernel(Params p) {
  __shared__ __attribute__((aligned(16))) float lds_[L_END];
  LAS float* lds = (LAS float*)lds_;
  const int tid = threadIdx.x, lane = tid & 63, w = __builtin_amdgcn_readfirstlane(tid >> 6);
  const int r = lane >> 4, c = lane & 15;
  const int bid = blockIdx.x, h = bid & 31, b = bid >> 5;
  const int hp = (h & 7) * 4 + (h >> 3);
  const __amdgpu_buffer_rsrc_t rw = __builtin_amdgcn_make_buffer_rsrc((void*)p.ws, 0, WS_END, RSRC_W3);
  v4u msk;
#pragma unroll
  for (int d = 0; d < 4; ++d) msk[d] = ((c >> 1) == d) ? ((c & 1) ? 0xffff0000u : 0x0000ffffu) : 0u;

  if (w < 4) {
    STAMP(0);
    const size_t kvo = ((size_t)bid * SK + (SK - WK)) * DH;
    const __amdgpu_buffer_rsrc_t rk = __builtin_amdgcn_make_buffer_rsrc((void*)(p.k + kvo), 0, WK * DH * 2, RSRC_W3);
    const __amdgpu_buffer_rsrc_t rv = __builtin_amdgcn_make_buffer_rsrc((void*)(p.v + kvo), 0, WK * DH * 2, RSRC_W3);
    const int vkv = w * 4096 + lane * 16;
    v4u kv[128];
    auto issue = [&](auto N) {
      constexpr int n = N.value;
      if constexpr (n < 128) kv[n] = __builtin_amdgcn_raw_buffer_load_b128(n < 64 ? rk : rv, vkv + (n & 3) * 1024, ((n >> 2) & 15) * 16384, AUXKV);
      fence();
    };
    static_for<0, DEPTH>([&](auto N) { issue(N); });
    static_for<DEPTH, HOLD>([&](auto N) { touch(kv[N.value - DEPTH]); fence(); issue(N); });
    STAMP(1);
    wg_barrier();
    STAMP(2);
    v4u qv;
    {
      f32x4 q0 = *(const LAS f32x4*)&lds[L_Q + 8 * c], q1 = *(const LAS f32x4*)&lds[L_Q + 8 * c + 4];
#pragma unroll
      for (int u = 1; u < 4; ++u) { q0 += *(const LAS f32x4*)&lds[L_Q + u * 128 + 8 * c]; q1 += *(const LAS f32x4*)&lds[L_Q + u * 128 + 8 * c + 4]; }
      qv = v4u{cvtpk(q0[0], q0[1]), cvtpk(q0[2], q0[3]), cvtpk(q1[0], q1[1]), cvtpk(q1[2], q1[3])};
    }
    float xs[16], sc[4];
    unsigned pw[4];
    f32x4 ao = {0.f, 0.f, 0.f, 0.f};
    auto consume = [&](auto I) {
      constexpr int i = I.value;
      if constexpr (i < 64) {
        float d = dot2(kv[i][0], qv[0], 0.f);
        d = dot2(kv[i][1], qv[1], d); d = dot2(kv[i][2], qv[2], d); d = dot2(kv[i][3], qv[3], d);
        xs[i & 15] = d;
        if constexpr ((i & 15) == 15) {
#define TR_STEP(HALF, CTRL) _Pragma("unroll") for (int u = 0; u < (HALF); ++u) { \
          const float lo_ = xs[u], hi_ = xs[u + (HALF)]; const float t1_ = lo_ + dppf<CTRL>(lo_), t2_ = hi_ + dppf<CTRL>(hi_); \
          xs[u] = (c & (HALF)) ? t2_ : t1_; }
          TR_STEP(8, XOR8) TR_STEP(4, XOR7) TR_STEP(2, XOR2) TR_STEP(1, XOR1)
#undef TR_STEP
          sc[i >> 4] = xs[0] * (0.08838834764831845f * 1.4426950408889634f);
        }
        if constexpr (i == 63) {
          const float mw = wave_max(fmaxf(fmaxf(sc[0], sc[1]), fmaxf(sc[2], sc[3])));
          float e[4];
#pragma unroll
          for (int u = 0; u < 4; ++u) { e[u] = __builtin_amdgcn_exp2f(sc[u] - mw); pw[u] = cvtpk(e[u], e[u]); }
          const float lw = wave_sum((e[0] + e[1]) + (e[2] + e[3]));
          if (lane == 0) *(LAS f32x2*)&lds[L_ST + 2 * w] = f32x2{mw, lw};
          lds_arrive(lds + L_CNT + 1, lane);
          STAMP(3);
        }
      } else {
        ao = rowmfma<(i & 15)>(pw[(i - 64) >> 4], msk, kv[i], ao);
      }
      fence();
    };
    static_for<HOLD, 128 + DEPTH>([&](auto N) {
      constexpr int n = N.value;
      constexpr int c0 = (n - HOLD) * RATE < n - DEPTH ? (n - HOLD) * RATE : n - DEPTH;
      constexpr int c1 = (n + 1 - HOLD) * RATE < n + 1 - DEPTH ? (n + 1 - HOLD) * RATE : n + 1 - DEPTH;
      static_for<c0, c1>([&](auto I) { consume(I); });
      if constexpr (c1 <= n - DEPTH) { touch(kv[n - DEPTH]); fence(); }
      issue(N);
    });
    STAMP(4);
    if (r < 2) *(LAS f32x4*)&lds[L_O + w * 128 + 8 * c + 4 * r] = ao;
    wg_barrier();
    if (w == 0) {
      const unsigned nonce = __float_as_uint(lds[L_CNT + 2]);
      const int slot = b * 32 + hp;
      const int od = WS_DATA + slot * 4096 + lane * 16, os = lane < 32 ? WS_SND + slot * 512 + lane * 16 : OOB;
      v4u d[4], s;
      bool ok[4] = {false, false, false, false}, oks = lane >= 32;
      for (int spin = 0; spin < SPIN_CAP; ++spin) {
#pragma unroll
        for (int q = 0; q < 4; ++q) if (!ok[q]) d[q] = __builtin_amdgcn_raw_buffer_load_b128(rw, od + q * 1024, 0, AUX_SC1);
        if (!oks) s = __builtin_amdgcn_raw_buffer_load_b128(rw, os, 0, AUX_SC1);
#pragma unroll
        for (int q = 0; q < 4; ++q) ok[q] = d[q][1] == nonce && d[q][3] == nonce;
        oks = lane >= 32 || s[1] == nonce;
        if (__all(ok[0] && ok[1] && ok[2] && ok[3] && oks)) break;
      }
      float s0 = (__uint_as_float(d[0][0]) + __uint_as_float(d[1][0])) + (__uint_as_float(d[2][0]) + __uint_as_float(d[3][0]));
      float s1 = (__uint_as_float(d[0][2]) + __uint_as_float(d[1][2])) + (__uint_as_float(d[2][2]) + __uint_as_float(d[3][2]));
      s0 += dppf<XOR8>(s0); s1 += dppf<XOR8>(s1);
      s0 = xrow_sum(s0); s1 = xrow_sum(s1);
      if (lane < 8) *(GAS unsigned*)((GAS unsigned short*)p.out + b * DM + hp * 16 + 2 * lane) = cvtpk(s0, s1);
      STAMP(5);
      const int g2 = (lane & 7) * 4 + ((lane >> 3) & 3);
      bool stale = false;
      if (lane < 32) { stale = __float_as_uint(lds[L_USED + g2]) != s[0]; lds[L_NEW + g2] = __uint_as_float(s[0]); }
      const bool any = __any(stale);
      if (lane == 0) lds[L_CNT + 3] = __uint_as_float(any ? 1u : 0u);
    }
    wg_barrier();
  } else {
    __builtin_amdgcn_s_setprio(3);
    const int ww = w - 4, j = h >> 3;
    const int ch0 = (NCH * hp) >> 5, nch = ((NCH * (hp + 1)) >> 5) - ch0;
    const __amdgpu_buffer_rsrc_t rq = __builtin_amdgcn_make_buffer_rsrc((void*)(p.wq), 0, DM * NH * DH * 2, RSRC_W3);
    const int vq = (128 * ww + r) * 8192 + h * 256 + c * 16;
    const GAS unsigned short* xb = (const GAS unsigned short*)p.x + b * DM;
    const unsigned xq0 = xb[128 * ww + 4 * ((c + 4 * b) & 31) + r], xq1 = xb[128 * ww + 4 * ((16 + c + 4 * b) & 31) + r];
    v4u wq[32], w1[16], w3[16], wo[32], w2[12];
    auto ldq = [&](auto I) { wq[I.value] = __builtin_amdgcn_raw_buffer_load_b128(rq, vq, ((I.value + 4 * b) & 31) * 32768, AUXW); fence(); };
    static_for<0, 4>(ldq);
    const __amdgpu_buffer_rsrc_t r1 = __builtin_amdgcn_make_buffer_rsrc((void*)(p.w1), 0, DM * FF * 2, RSRC_W3);
    const __amdgpu_buffer_rsrc_t r3 = __builtin_amdgcn_make_buffer_rsrc((void*)(p.w3), 0, DM * FF * 2, RSRC_W3);
    const __amdgpu_buffer_rsrc_t ro = __builtin_amdgcn_make_buffer_rsrc((void*)(p.wo + (size_t)h * DH * DM), 0, DH * DM * 2, RSRC_W3);
    const __amdgpu_buffer_rsrc_t r2 = __builtin_amdgcn_make_buffer_rsrc((void*)(p.w2 + (size_t)ch0 * 8 * DM), 0, nch * 8 * DM * 2, RSRC_W3);
    const int v13 = ((c & 7) < nch) ? (128 * ww + 2 * r + (c >> 3)) * (FF * 2) + (ch0 + (c & 7)) * 16 : OOB;
    const int vo = r * 1024 + ww * 256 + c * 16;
    const int rot2 = (3 * b) >> 1;
    auto ld13 = [&](auto I) {
      w1[I.value] = __builtin_amdgcn_raw_buffer_load_b128(r1, v13, ((I.value + 2 * b) & 15) * (8 * FF * 2), AUXW);
      w3[I.value] = __builtin_amdgcn_raw_buffer_load_b128(r3, v13, ((I.value + 2 * b) & 15) * (8 * FF * 2), AUXW);
      fence();
    };
    auto ldo = [&](auto I) { wo[I.value] = __builtin_amdgcn_raw_buffer_load_b128(ro, vo, ((I.value + 4 * b) & 31) * 4096, AUXW); fence(); };
    auto ld2 = [&](auto I) { const int s = I.value + rot2; w2[I.value] = __builtin_amdgcn_raw_buffer_load_b128(r2, vo, (s >= 12 ? s - 12 : s) * 4096, AUXW); fence(); };
    const unsigned xp = *(const GAS unsigned*)(xb + 128 * ww + 8 * ((c + 2 * b) & 15) + 2 * r);
    const v4u xg = *(const GAS v4u*)(xb + 8 * lane);
    const v4u gw = *(const GAS v4u*)((const GAS unsigned short*)p.gw + 8 * lane);
    fence();
    static_for<0, 2>(ld13);
    static_for<0, 2>(ld2);
    static_for<0, 4>(ldo);
    if (ww == 3) {
      const int slot = b * 32 + hp;
      const int od = WS_DATA + slot * 4096 + lane * 16, os = lane < 32 ? WS_SND + slot * 512 + lane * 16 : OOB;
      const unsigned old = __builtin_amdgcn_raw_buffer_load_b32(rw, 0, WS_R + slot * 128, AUX_SC1);
      v4u d[5];
#pragma unroll
      for (int q = 0; q < 4; ++q) d[q] = __builtin_amdgcn_raw_buffer_load_b128(rw, od + q * 1024, 0, AUX_SC1);
      d[4] = __builtin_amdgcn_raw_buffer_load_b128(rw, os, 0, AUX_SC1);
      unsigned nonce = __builtin_amdgcn_readfirstlane(old);
      for (;;) {
        ++nonce;
        bool hit = false;
#pragma unroll
        for (int q = 0; q < 5; ++q) hit |= d[q][1] == nonce || d[q][3] == nonce;
        if (!__any(hit)) break;
      }
      if (lane == 0) {
        __builtin_amdgcn_raw_buffer_store_b32(nonce, rw, 0, WS_R + slot * 128, AUX_SC1);
        lds[L_CNT + 0] = 0.f; lds[L_CNT + 1] = 0.f; lds[L_CNT + 2] = __uint_as_float(nonce);
      }
      asm volatile("s_waitcnt vmcnt(0)" ::: "memory");
    }
    v4u sel;
    {
      const unsigned hb = (c >> 3) ? 0x0302u : 0x0100u;
      const unsigned s = (c & 1) ? ((hb << 16) | 0x0c0cu) : (0x0c0c0000u | hb);
#pragma unroll
      for (int d = 0; d < 4; ++d) sel[d] = (((c & 7) >> 1) == d) ? s : 0x0c0c0c0cu;
    }
    const unsigned xq[2] = {xq0 | (xq0 << 16), xq1 | (xq1 << 16)};
    f32x4 aq = {0.f, 0.f, 0.f, 0.f}, ag = aq, au = aq, af = aq;
    auto useq = [&](auto I) { aq = rowmfma<(I.value & 15)>(xq[I.value >> 4], msk, wq[I.value], aq); fence(); };
    auto use13 = [&](auto I) { const v4u a = pair_a<I.value>(xp, sel); ag = mfma(a, w1[I.value], ag); au = mfma(a, w3[I.value], au); fence(); };
    nap(T1 + S1 * j);
    fence();
    static_for<4, 32>(ldq);
    static_for<0, 4>(useq);
    static_for<0, 2>(use13);
    float g;
    {
      float d = dot2(xg[0], gw[0], 0.f);
      d = dot2(xg[1], gw[1], d); d = dot2(xg[2], gw[2], d); d = dot2(xg[3], gw[3], d);
      d = wave_sum(d);
      g = 1.f / (1.f + __expf(-d));
      asm volatile("" : "+v"(g));
    }
    static_for<4, 32>(useq);
    if (r < 2) *(LAS f32x4*)&lds[L_Q + ww * 128 + 8 * c + 4 * r] = aq;
    STAMP(6);
    wg_barrier();
    nap(T2 + S2 * j);
    fence();
    static_for<2, 16>(ld13);
    static_for<2, 16>(use13);
    if ((r >> 1) == (c >> 3)) {
      *(LAS f32x4*)&lds[L_GU + ww * 256 + (r >> 1) * 128 + 8 * (c & 7) + 4 * (r & 1)] = ag;
      *(LAS f32x4*)&lds[L_GU + ww * 256 + (r >> 1) * 128 + 64 + 8 * (c & 7) + 4 * (r & 1)] = au;
    }
    lds_arrive(lds + L_CNT + 0, lane);
    lds_wait4(lds + L_CNT + 0);
    STAMP(7);
    unsigned hw;
    {
      const int s2 = c + rot2 >= 12 ? c + rot2 - 12 : c + rot2;
      const int f = (4 * s2 + r) & 63;
      float gt = 0.f, up = 0.f;
#pragma unroll
      for (int u = 0; u < 8; ++u) { gt += lds[L_GU + u * 128 + f]; up += lds[L_GU + u * 128 + 64 + f]; }
      const float hv = gt / (1.f + __expf(-gt)) * up;
      hw = (c < 12 && f < 8 * nch) ? cvtpk(hv, hv) : 0u;
    }
    lds_wait4(lds + L_CNT + 1);
    float fw[4];
    {
      f32x2 st[4];
#pragma unroll
      for (int u = 0; u < 4; ++u) st[u] = *(const LAS f32x2*)&lds[L_ST + 2 * u];
      const float M = fmaxf(fmaxf(st[0][0], st[1][0]), fmaxf(st[2][0], st[3][0]));
      float L = 0.f;
#pragma unroll
      for (int u = 0; u < 4; ++u) { fw[u] = __builtin_amdgcn_exp2f(st[u][0] - M); L += fw[u] * st[u][1]; }
      const float inv = g / L;
#pragma unroll
      for (int u = 0; u < 4; ++u) { fw[u] *= inv; asm volatile("" : "+v"(fw[u])); }
    }
    nap(T3 + S3 * j);
    fence();
    static_for<2, 12>(ld2);
    static_for<4, 32>(ldo);
    const int cg = 8 * ww + (c >> 1);
    unsigned tag = __builtin_amdgcn_raw_buffer_load_b32(rw, WS_R + (b * 32 + cg) * 128, 0, AUX_SC1);
    fence();
    static_for<0, 12>([&](auto I) { af = rowmfma<I.value>(hw, msk, w2[I.value], af); fence(); });
    af *= (1.f - g);
    if (r == 3 && (c & 1) == 0) lds[L_USED + cg] = __uint_as_float(tag);
    const unsigned mine = __float_as_uint(lds[L_CNT + 2]);
    const int slot = b * 32 + cg;
    const int o1 = r < 2 ? WS_DATA + slot * 4096 + h * 128 + (8 * (c & 1) + 4 * r) * 8 : (r == 2 && (c & 1) == 0) ? WS_SND + slot * 512 + h * 16 : OOB;
    const int o2 = r < 2 ? o1 + 16 : OOB;
    const int lo0 = L_O + 4 * ((c + 4 * b) & 31) + r, lo1 = L_O + 4 * ((16 + c + 4 * b) & 31) + r;
    static_for<0, 32>([&](auto I) { touch(wo[I.value]); });
    asm volatile("" : "+v"(af));
    STAMP(8);
    wg_barrier();
    STAMP(9);
    float o0 = lds[lo0] * fw[0], o1v = lds[lo1] * fw[0];
#pragma unroll
    for (int u = 1; u < 4; ++u) { o0 = fmaf(lds[lo0 + u * 128], fw[u], o0); o1v = fmaf(lds[lo1 + u * 128], fw[u], o1v); }
    const unsigned ow[2] = {cvtpk(o0, o0), cvtpk(o1v, o1v)};
    static_for<0, 32>([&](auto I) { af = rowmfma<(I.value & 15)>(ow[I.value >> 4], msk, wo[I.value], af); });
    v4u m1 = {r < 2 ? __float_as_uint(af[0]) : mine, tag, r < 2 ? __float_as_uint(af[1]) : mine, tag};
    v4u m2 = {__float_as_uint(af[2]), tag, __float_as_uint(af[3]), tag};
    __builtin_amdgcn_raw_buffer_store_b128(m1, rw, o1, 0, AUX_SC1);
    __builtin_amdgcn_raw_buffer_store_b128(m2, rw, o2, 0, AUX_SC1);
    STAMP(10);
    wg_barrier();
    if (__float_as_uint(lds[L_CNT + 3]) != 0u) {
      tag = __float_as_uint(lds[L_NEW + cg]);
      m1[1] = tag; m1[3] = tag; m2[1] = tag; m2[3] = tag;
      __builtin_amdgcn_raw_buffer_store_b128(m1, rw, o1, 0, AUX_SC1);
      __builtin_amdgcn_raw_buffer_store_b128(m2, rw, o2, 0, AUX_SC1);
    }
  }
}

extern "C" void kernel_launch(void* const* d_in, const int* in_sizes, int n_in, void* d_out, int out_size, void* d_ws,
                              size_t ws_size, hipStream_t stream) {
  Params p;
  p.x = (const unsigned short*)d_in[0];
  p.k = (const unsigned short*)d_in[1];
  p.v = (const unsigned short*)d_in[2];
  p.wq = (const unsigned short*)d_in[3];
  p.wo = (const unsigned short*)d_in[4];
  p.w1 = (const unsigned short*)d_in[6];
  p.w3 = (const unsigned short*)d_in[7];
  p.w2 = (const unsigned short*)d_in[8];
  p.gw = (const unsigned short*)d_in[9];
  p.out = (unsigned short*)d_out;
  p.ws = (unsigned char*)d_ws;
  hipLaunchKernelGGL(fused_kernel, dim3(NB * NH), dim3(NTHR), 0, stream, p);
}
```

```cpp
#include <hip/hip_runtime.h>
#include <hip/hip_bf16.h>
#include <stdint.h>

typedef unsigned v4u __attribute__((ext_vector_type(4)));
typedef float f32x4 __attribute__((ext_vector_type(4)));
typedef float f32x2 __attribute__((ext_vector_type(2)));
typedef __bf16 bf16x8 __attribute__((ext_vector_type(8)));
typedef __bf16 bf16x2 __attribute__((ext_vector_type(2)));
#define GAS __attribute__((address_space(1)))
#define LAS __attribute__((address_space(3)))
#define DEVI __device__ __forceinline__

#define DEPTH 8
#define HOLD 48
#define RATE 4
#define AUXKV 2
#define AUXW 0
#define T1 3
#define S1 3
#define T2 0
#define S2 5
#define T3 0
#define S3 4
#define NSTAMP 12
#define PROBE0 0
#define PROBE 0
#define PK 0
#define STAMP(i) do { __builtin_amdgcn_sched_barrier(0); const unsigned long long t_ = __builtin_amdgcn_s_memrealtime(); __builtin_amdgcn_s_waitcnt(0xC07F); \
  if (lane == 0 && (w & 3) == 0) ((LAS unsigned long long*)(lds + L_STAMP))[i] = t_; __builtin_amdgcn_sched_barrier(0); } while (0)

constexpr int NB = 8, DM = 512, NH = 32, SK = 4096, DH = 128, WK = 1024, FF = 1376;
constexpr int NCH = FF / 8;
constexpr int NTHR = 512;
constexpr int RSRC_W3 = 0x00020000;
constexpr int AUX_SC1 = 16;
constexpr int OOB = 0x40000000;
constexpr int WS_R = 0, WS_DATA = 256 * 128, WS_SND = WS_DATA + 256 * 4096, WS_END = WS_SND + 256 * 512;
constexpr int SPIN_CAP = 1 << 18;

template <int N> struct IC { static constexpr int value = N; };
template <int LO, int HI, class Fn> DEVI void static_for(Fn&& f) {
  if constexpr (LO < HI) { f(IC<LO>{}); static_for<LO + 1, HI>(f); }
}
DEVI float dot2(unsigned a, unsigned b, float c) {
  return __builtin_amdgcn_fdot2_f32_bf16(__builtin_bit_cast(bf16x2, a), __builtin_bit_cast(bf16x2, b), c, false);
}
DEVI unsigned cvtpk(float lo, float hi) {
  bf16x2 r = {(__bf16)lo, (__bf16)hi};
  return __builtin_bit_cast(unsigned, r);
}
template <int CTRL> DEVI float dppf(float x) {
  return __builtin_bit_cast(float, __builtin_amdgcn_mov_dpp(__builtin_bit_cast(int, x), CTRL, 0xf, 0xf, true));
}
template <int CTRL> DEVI unsigned dppu(unsigned x) { return (unsigned)__builtin_amdgcn_mov_dpp((int)x, CTRL, 0xf, 0xf, true); }
constexpr int XOR1 = 0xB1, XOR2 = 0x4E, XOR7 = 0x141, XOR8 = 0x128, BCAST = 0x150;
DEVI float xrow_max(float x) {
  auto s = __builtin_amdgcn_permlane16_swap(__float_as_uint(x), __float_as_uint(x), false, false);
  x = fmaxf(__uint_as_float(s[0]), __uint_as_float(s[1]));
  auto t = __builtin_amdgcn_permlane32_swap(__float_as_uint(x), __float_as_uint(x), false, false);
  return fmaxf(__uint_as_float(t[0]), __uint_as_float(t[1]));
}
DEVI float xrow_sum(float x) {
  auto s = __builtin_amdgcn_permlane16_swap(__float_as_uint(x), __float_as_uint(x), false, false);
  x = __uint_as_float(s[0]) + __uint_as_float(s[1]);
  auto t = __builtin_amdgcn_permlane32_swap(__float_as_uint(x), __float_as_uint(x), false, false);
  return __uint_as_float(t[0]) + __uint_as_float(t[1]);
}
DEVI float wave_max(float x) {
  x = fmaxf(x, dppf<XOR1>(x)); x = fmaxf(x, dppf<XOR2>(x)); x = fmaxf(x, dppf<XOR7>(x)); x = fmaxf(x, dppf<XOR8>(x));
  return xrow_max(x);
}
DEVI float wave_sum(float x) {
  x += dppf<XOR1>(x); x += dppf<XOR2>(x); x += dppf<XOR7>(x); x += dppf<XOR8>(x);
  return xrow_sum(x);
}
DEVI f32x4 mfma(v4u a, v4u b, f32x4 acc) {
  return __builtin_amdgcn_mfma_f32_16x16x32_bf16(__builtin_bit_cast(bf16x8, a), __builtin_bit_cast(bf16x8, b), acc, 0, 0, 0);
}
template <int T> DEVI v4u pair_a(unsigned word, const v4u& sel) {
  const unsigned wb = dppu<BCAST + T>(word);
  return v4u{__builtin_amdgcn_perm(wb, wb, sel[0]), __builtin_amdgcn_perm(wb, wb, sel[1]),
             __builtin_amdgcn_perm(wb, wb, sel[2]), __builtin_amdgcn_perm(wb, wb, sel[3])};
}
template <int T> DEVI f32x4 rowmfma(unsigned word, const v4u& msk, v4u b, f32x4 acc) {
  v4u a = {dppu<BCAST + T>(word) & msk[0], dppu<BCAST + T>(word) & msk[1], dppu<BCAST + T>(word) & msk[2], dppu<BCAST + T>(word) & msk[3]};
  return mfma(a, b, acc);
}
DEVI void touch(const v4u& x) { asm volatile("" :: "v"(x)); }
DEVI void fence() { __builtin_amdgcn_sched_barrier(0); }
DEVI void wg_barrier() { asm volatile("s_waitcnt lgkmcnt(0)\n\ts_barrier" ::: "memory"); }
DEVI void nap(int n) { for (int i = 0; i < n; ++i) __builtin_amdgcn_s_sleep(8); }
DEVI void lds_arrive(LAS float* p, int lane) {
  asm volatile("" ::: "memory");
  if (lane == 0) __hip_atomic_fetch_add((LAS unsigned*)p, 1u, __ATOMIC_RELAXED, __HIP_MEMORY_SCOPE_WORKGROUP);
}
DEVI void lds_wait4(LAS float* p) {
  while (__hip_atomic_load((LAS unsigned*)p, __ATOMIC_RELAXED, __HIP_MEMORY_SCOPE_WORKGROUP) < 4u) __builtin_amdgcn_s_sleep(1);
  asm volatile("" ::: "memory");
}

struct Params {
  const unsigned short *x, *k, *v, *wq, *wo, *w1, *w3, *w2, *gw;
  unsigned short* out;
  unsigned char* ws;
};

constexpr int L_Q = 0;
constexpr int L_GU = L_Q + 4 * 128;
constexpr int L_O = L_GU + 4 * 256;
constexpr int L_ST = L_O + 4 * 128;
constexpr int L_USED = L_ST + 8;
constexpr int L_NEW = L_USED + 32;
constexpr int L_CNT = L_NEW + 32;
constexpr int L_STAMP = L_CNT + 4;
constexpr int L_END = L_STAMP + 2 * 16;

__global__ void __launch_bounds__(NTHR, 2) __attribute__((amdgpu_waves_per_eu(2, 2))) fused_kernel(Params p) {
  __shared__ __attribute__((aligned(16))) float lds_[L_END];
  LAS float* lds = (LAS float*)lds_;
  const int tid = threadIdx.x, lane = tid & 63, w = __builtin_amdgcn_readfirstlane(tid >> 6);
  const int r = lane >> 4, c = lane & 15;
  const int bid = blockIdx.x, h = bid & 31, b = bid >> 5;
  const int hp = (h & 7) * 4 + (h >> 3);
  const __amdgpu_buffer_rsrc_t rw = __builtin_amdgcn_make_buffer_rsrc((void*)p.ws, 0, WS_END, RSRC_W3);
  v4u msk;
#pragma unroll
  for (int d = 0; d < 4; ++d) msk[d] = ((c >> 1) == d) ? ((c & 1) ? 0xffff0000u : 0x0000ffffu) : 0u;

  if (w < 4) {
    STAMP(0);
    const size_t kvo = ((size_t)bid * SK + (SK - WK)) * DH;
    const __amdgpu_buffer_rsrc_t rk = __builtin_amdgcn_make_buffer_rsrc((void*)(p.k + kvo), 0, WK * DH * 2, RSRC_W3);
    const __amdgpu_buffer_rsrc_t rv = __builtin_amdgcn_make_buffer_rsrc((void*)(p.v + kvo), 0, WK * DH * 2, RSRC_W3);
    const int vkv = w * 4096 + lane * 16;
    v4u kv[128];
    auto issue = [&](auto N) {
      constexpr int n = N.value;
      if constexpr (n < 128) kv[n] = __builtin_amdgcn_raw_buffer_load_b128(n < 64 ? rk : rv, vkv + (n & 3) * 1024, ((n >> 2) & 15) * 16384, AUXKV);
      fence();
    };
    static_for<0, DEPTH>([&](auto N) { issue(N); });
    touch(kv[0]); STAMP(11);
    static_for<DEPTH, HOLD>([&](auto N) { touch(kv[N.value - DEPTH]); fence(); issue(N); });
    STAMP(1);
    wg_barrier();
    STAMP(2);
    v4u qv;
    {
      f32x4 q0 = *(const LAS f32x4*)&lds[L_Q + 8 * c], q1 = *(const LAS f32x4*)&lds[L_Q + 8 * c + 4];
#pragma unroll
      for (int u = 1; u < 4; ++u) { q0 += *(const LAS f32x4*)&lds[L_Q + u * 128 + 8 * c]; q1 += *(const LAS f32x4*)&lds[L_Q + u * 128 + 8 * c + 4]; }
      qv = v4u{cvtpk(q0[0], q0[1]), cvtpk(q0[2], q0[3]), cvtpk(q1[0], q1[1]), cvtpk(q1[2], q1[3])};
    }
    float xs[16], sc[4];
    unsigned pw[4];
    f32x4 ao = {0.f, 0.f, 0.f, 0.f};
    auto consume = [&](auto I) {
      constexpr int i = I.value;
      if constexpr (i < 64) {
        float d = dot2(kv[i][0], qv[0], 0.f);
        d = dot2(kv[i][1], qv[1], d); d = dot2(kv[i][2], qv[2], d); d = dot2(kv[i][3], qv[3], d);
        xs[i & 15] = d;
        if constexpr ((i & 15) == 15) {
#define TR_STEP(HALF, CTRL) _Pragma("unroll") for (int u = 0; u < (HALF); ++u) { \
          const float lo_ = xs[u], hi_ = xs[u + (HALF)]; const float t1_ = lo_ + dppf<CTRL>(lo_), t2_ = hi_ + dppf<CTRL>(hi_); \
          xs[u] = (c & (HALF)) ? t2_ : t1_; }
          TR_STEP(8, XOR8) TR_STEP(4, XOR7) TR_STEP(2, XOR2) TR_STEP(1, XOR1)
#undef TR_STEP
          sc[i >> 4] = xs[0] * (0.08838834764831845f * 1.4426950408889634f);
        }
        if constexpr (i == 63) {
          const float mw = wave_max(fmaxf(fmaxf(sc[0], sc[1]), fmaxf(sc[2], sc[3])));
          float e[4];
#pragma unroll
          for (int u = 0; u < 4; ++u) { e[u] = __builtin_amdgcn_exp2f(sc[u] - mw); pw[u] = cvtpk(e[u], e[u]); }
          const float lw = wave_sum((e[0] + e[1]) + (e[2] + e[3]));
          if (lane == 0) *(LAS f32x2*)&lds[L_ST + 2 * w] = f32x2{mw, lw};
          lds_arrive(lds + L_CNT + 1, lane);
          STAMP(3);
        }
      } else {
        ao = rowmfma<(i & 15)>(pw[(i - 64) >> 4], msk, kv[i], ao);
      }
      fence();
    };
    static_for<HOLD, 128 + DEPTH>([&](auto N) {
      constexpr int n = N.value;
      constexpr int c0 = (n - HOLD) * RATE < n - DEPTH ? (n - HOLD) * RATE : n - DEPTH;
      constexpr int c1 = (n + 1 - HOLD) * RATE < n + 1 - DEPTH ? (n + 1 - HOLD) * RATE : n + 1 - DEPTH;
      static_for<c0, c1>([&](auto I) { consume(I); });
      if constexpr (c1 <= n - DEPTH) { touch(kv[n - DEPTH]); fence(); }
      issue(N);
    });
    STAMP(4);
    if (r < 2) *(LAS f32x4*)&lds[L_O + w * 128 + 8 * c + 4 * r] = ao;
    wg_barrier();
    if (w == 0) {
      const unsigned nonce = __float_as_uint(lds[L_CNT + 2]);
      const int slot = b * 32 + hp;
      const int od = WS_DATA + slot * 4096 + lane * 16, os = lane < 32 ? WS_SND + slot * 512 + lane * 16 : OOB;
      v4u d[4], s;
      bool ok[4] = {false, false, false, false}, oks = lane >= 32;
      for (int spin = 0; spin < SPIN_CAP; ++spin) {
#pragma unroll
        for (int q = 0; q < 4; ++q) if (!ok[q]) d[q] = __builtin_amdgcn_raw_buffer_load_b128(rw, od + q * 1024, 0, AUX_SC1);
        if (!oks) s = __builtin_amdgcn_raw_buffer_load_b128(rw, os, 0, AUX_SC1);
#pragma unroll
        for (int q = 0; q < 4; ++q) ok[q] = d[q][1] == nonce && d[q][3] == nonce;
        oks = lane >= 32 || s[1] == nonce;
        if (__all(ok[0] && ok[1] && ok[2] && ok[3] && oks)) break;
      }
      float s0 = (__uint_as_float(d[0][0]) + __uint_as_float(d[1][0])) + (__uint_as_float(d[2][0]) + __uint_as_float(d[3][0]));
      float s1 = (__uint_as_float(d[0][2]) + __uint_as_float(d[1][2])) + (__uint_as_float(d[2][2]) + __uint_as_float(d[3][2]));
      s0 += dppf<XOR8>(s0); s1 += dppf<XOR8>(s1);
      s0 = xrow_sum(s0); s1 = xrow_sum(s1);
      if (lane < 8) *(GAS unsigned*)((GAS unsigned short*)p.out + b * DM + hp * 16 + 2 * lane) = cvtpk(s0, s1);
      STAMP(5);
      const int g2 = (lane & 7) * 4 + ((lane >> 3) & 3);
      bool stale = false;
      if (lane < 32) { stale = __float_as_uint(lds[L_USED + g2]) != s[0]; lds[L_NEW + g2] = __uint_as_float(s[0]); }
      const bool any = __any(stale);
      if (lane == 0) lds[L_CNT + 3] = __uint_as_float(any ? 1u : 0u);
    }
    wg_barrier();
  } else {
    __builtin_amdgcn_s_setprio(3);
    const int ww = w - 4, j = h >> 3;
    const int ch0 = (NCH * hp) >> 5, nch = ((NCH * (hp + 1)) >> 5) - ch0;
    const __amdgpu_buffer_rsrc_t rq = __builtin_amdgcn_make_buffer_rsrc((void*)(p.wq), 0, DM * NH * DH * 2, RSRC_W3);
    const int vq = (128 * ww + r) * 8192 + h * 256 + c * 16;
    const GAS unsigned short* xb = (const GAS unsigned short*)p.x + b * DM;
    const unsigned xq0 = xb[128 * ww + 4 * ((c + 4 * b) & 31) + r], xq1 = xb[128 * ww + 4 * ((16 + c + 4 * b) & 31) + r];
    v4u wq[32], w1[16], w3[16], wo[32], w2[12];
    auto ldq = [&](auto I) { wq[I.value] = __builtin_amdgcn_raw_buffer_load_b128(rq, vq, ((I.value + 4 * b) & 31) * 32768, AUXW); fence(); };
    static_for<0, 4>(ldq);
    const __amdgpu_buffer_rsrc_t r1 = __builtin_amdgcn_make_buffer_rsrc((void*)(p.w1), 0, DM * FF * 2, RSRC_W3);
    const __amdgpu_buffer_rsrc_t r3 = __builtin_amdgcn_make_buffer_rsrc((void*)(p.w3), 0, DM * FF * 2, RSRC_W3);
    const __amdgpu_buffer_rsrc_t ro = __builtin_amdgcn_make_buffer_rsrc((void*)(p.wo + (size_t)h * DH * DM), 0, DH * DM * 2, RSRC_W3);
    const __amdgpu_buffer_rsrc_t r2 = __builtin_amdgcn_make_buffer_rsrc((void*)(p.w2 + (size_t)ch0 * 8 * DM), 0, nch * 8 * DM * 2, RSRC_W3);
    const int v13 = ((c & 7) < nch) ? (128 * ww + 2 * r + (c >> 3)) * (FF * 2) + (ch0 + (c & 7)) * 16 : OOB;
    const int vo = r * 1024 + ww * 256 + c * 16;
    const int rot2 = (3 * b) >> 1;
    auto ld13 = [&](auto I) {
      w1[I.value] = __builtin_amdgcn_raw_buffer_load_b128(r1, v13, ((I.value + 2 * b) & 15) * (8 * FF * 2), AUXW);
      w3[I.value] = __builtin_amdgcn_raw_buffer_load_b128(r3, v13, ((I.value + 2 * b) & 15) * (8 * FF * 2), AUXW);
      fence();
    };
    auto ldo = [&](auto I) { wo[I.value] = __builtin_amdgcn_raw_buffer_load_b128(ro, vo, ((I.value + 4 * b) & 31) * 4096, AUXW); fence(); };
    auto ld2 = [&](auto I) { const int s = I.value + rot2; w2[I.value] = __builtin_amdgcn_raw_buffer_load_b128(r2, vo, (s >= 12 ? s - 12 : s) * 4096, AUXW); fence(); };
    const unsigned xp = *(const GAS unsigned*)(xb + 128 * ww + 8 * ((c + 2 * b) & 15) + 2 * r);
    const v4u xg = *(const GAS v4u*)(xb + 8 * lane);
    const v4u gw = *(const GAS v4u*)((const GAS unsigned short*)p.gw + 8 * lane);
    fence();
    static_for<0, 2>(ld13);
    static_for<0, 2>(ld2);
    static_for<0, 4>(ldo);
    if (ww == 3) {
      const int slot = b * 32 + hp;
      const int od = WS_DATA + slot * 4096 + lane * 16, os = lane < 32 ? WS_SND + slot * 512 + lane * 16 : OOB;
      const unsigned old = __builtin_amdgcn_raw_buffer_load_b32(rw, 0, WS_R + slot * 128, AUX_SC1);
      v4u d[5];
#pragma unroll
      for (int q = 0; q < 4; ++q) d[q] = __builtin_amdgcn_raw_buffer_load_b128(rw, od + q * 1024, 0, AUX_SC1);
      d[4] = __builtin_amdgcn_raw_buffer_load_b128(rw, os, 0, AUX_SC1);
      unsigned nonce = __builtin_amdgcn_readfirstlane(old);
      for (;;) {
        ++nonce;
        bool hit = false;
#pragma unroll
        for (int q = 0; q < 5; ++q) hit |= d[q][1] == nonce || d[q][3] == nonce;
        if (!__any(hit)) break;
      }
      if (lane == 0) {
        __builtin_amdgcn_raw_buffer_store_b32(nonce, rw, 0, WS_R + slot * 128, AUX_SC1);
        lds[L_CNT + 0] = 0.f; lds[L_CNT + 1] = 0.f; lds[L_CNT + 2] = __uint_as_float(nonce);
      }
      asm volatile("s_waitcnt vmcnt(0)" ::: "memory");
    }
    v4u sel;
    {
      const unsigned hb = (c >> 3) ? 0x0302u : 0x0100u;
      const unsigned s = (c & 1) ? ((hb << 16) | 0x0c0cu) : (0x0c0c0000u | hb);
#pragma unroll
      for (int d = 0; d < 4; ++d) sel[d] = (((c & 7) >> 1) == d) ? s : 0x0c0c0c0cu;
    }
    const unsigned xq[2] = {xq0 | (xq0 << 16), xq1 | (xq1 << 16)};
    f32x4 aq = {0.f, 0.f, 0.f, 0.f}, ag = aq, au = aq, af = aq;
    auto useq = [&](auto I) { aq = rowmfma<(I.value & 15)>(xq[I.value >> 4], msk, wq[I.value], aq); fence(); };
    auto use13 = [&](auto I) { const v4u a = pair_a<I.value>(xp, sel); ag = mfma(a, w1[I.value], ag); au = mfma(a, w3[I.value], au); fence(); };
    nap(T1 + S1 * j);
    fence();
    static_for<4, 32>(ldq);
    static_for<0, 4>(useq);
    static_for<0, 2>(use13);
    float g;
    {
      float d = dot2(xg[0], gw[0], 0.f);
      d = dot2(xg[1], gw[1], d); d = dot2(xg[2], gw[2], d); d = dot2(xg[3], gw[3], d);
      d = wave_sum(d);
      g = 1.f / (1.f + __expf(-d));
      asm volatile("" : "+v"(g));
    }
    static_for<4, 32>(useq);
    if (r < 2) *(LAS f32x4*)&lds[L_Q + ww * 128 + 8 * c + 4 * r] = aq;
    STAMP(6);
    wg_barrier();
    nap(T2 + S2 * j);
    fence();
    static_for<2, 16>(ld13);
    static_for<2, 16>(use13);
    if ((r >> 1) == (c >> 3)) {
      *(LAS f32x4*)&lds[L_GU + ww * 256 + (r >> 1) * 128 + 8 * (c & 7) + 4 * (r & 1)] = ag;
      *(LAS f32x4*)&lds[L_GU + ww * 256 + (r >> 1) * 128 + 64 + 8 * (c & 7) + 4 * (r & 1)] = au;
    }
    lds_arrive(lds + L_CNT + 0, lane);
    lds_wait4(lds + L_CNT + 0);
    STAMP(7);
    unsigned hw;
    {
      const int s2 = c + rot2 >= 12 ? c + rot2 - 12 : c + rot2;
      const int f = (4 * s2 + r) & 63;
      float gt = 0.f, up = 0.f;
#pragma unroll
      for (int u = 0; u < 8; ++u) { gt += lds[L_GU + u * 128 + f]; up += lds[L_GU + u * 128 + 64 + f]; }
      const float hv = gt / (1.f + __expf(-gt)) * up;
      hw = (c < 12 && f < 8 * nch) ? cvtpk(hv, hv) : 0u;
    }
    lds_wait4(lds + L_CNT + 1);
    float fw[4];
    {
      f32x2 st[4];
#pragma unroll
      for (int u = 0; u < 4; ++u) st[u] = *(const LAS f32x2*)&lds[L_ST + 2 * u];
      const float M = fmaxf(fmaxf(st[0][0], st[1][0]), fmaxf(st[2][0], st[3][0]));
      float L = 0.f;
#pragma unroll
      for (int u = 0; u < 4; ++u) { fw[u] = __builtin_amdgcn_exp2f(st[u][0] - M); L += fw[u] * st[u][1]; }
      const float inv = g / L;
#pragma unroll
      for (int u = 0; u < 4; ++u) { fw[u] *= inv; asm volatile("" : "+v"(fw[u])); }
    }
    nap(T3 + S3 * j);
    fence();
    static_for<2, 12>(ld2);
    static_for<4, 32>(ldo);
    const int cg = 8 * ww + (c >> 1);
    unsigned tag = __builtin_amdgcn_raw_buffer_load_b32(rw, WS_R + (b * 32 + cg) * 128, 0, AUX_SC1);
    fence();
    static_for<0, 12>([&](auto I) { af = rowmfma<I.value>(hw, msk, w2[I.value], af); fence(); });
    af *= (1.f - g);
    if (r == 3 && (c & 1) == 0) lds[L_USED + cg] = __uint_as_float(tag);
    const unsigned mine = __float_as_uint(lds[L_CNT + 2]);
    const int slot = b * 32 + cg;
    const int o1 = r < 2 ? WS_DATA + slot * 4096 + h * 128 + (8 * (c & 1) + 4 * r) * 8 : (r == 2 && (c & 1) == 0) ? WS_SND + slot * 512 + h * 16 : OOB;
    const int o2 = r < 2 ? o1 + 16 : OOB;
    const int lo0 = L_O + 4 * ((c + 4 * b) & 31) + r, lo1 = L_O + 4 * ((16 + c + 4 * b) & 31) + r;
    static_for<0, 32>([&](auto I) { touch(wo[I.value]); });
    asm volatile("" : "+v"(af));
    STAMP(8);
    wg_barrier();
    STAMP(9);
    float o0 = lds[lo0] * fw[0], o1v = lds[lo1] * fw[0];
#pragma unroll
    for (int u = 1; u < 4; ++u) { o0 = fmaf(lds[lo0 + u * 128], fw[u], o0); o1v = fmaf(lds[lo1 + u * 128], fw[u], o1v); }
    const unsigned ow[2] = {cvtpk(o0, o0), cvtpk(o1v, o1v)};
    static_for<0, 32>([&](auto I) { af = rowmfma<(I.value & 15)>(ow[I.value >> 4], msk, wo[I.value], af); });
    v4u m1 = {r < 2 ? __float_as_uint(af[0]) : mine, tag, r < 2 ? __float_as_uint(af[1]) : mine, tag};
    v4u m2 = {__float_as_uint(af[2]), tag, __float_as_uint(af[3]), tag};
    __builtin_amdgcn_raw_buffer_store_b128(m1, rw, o1, 0, AUX_SC1);
    __builtin_amdgcn_raw_buffer_store_b128(m2, rw, o2, 0, AUX_SC1);
    STAMP(10);
    wg_barrier();
    if (__float_as_uint(lds[L_CNT + 3]) != 0u) {
      tag = __float_as_uint(lds[L_NEW + cg]);
      m1[1] = tag; m1[3] = tag; m2[1] = tag; m2[3] = tag;
      __builtin_amdgcn_raw_buffer_store_b128(m1, rw, o1, 0, AUX_SC1);
      __builtin_amdgcn_raw_buffer_store_b128(m2, rw, o2, 0, AUX_SC1);
    }
  }
  if (w == 0) {
    const unsigned long long a_ = ((LAS unsigned long long*)(lds + L_STAMP))[PROBE0], b_ = ((LAS unsigned long long*)(lds + L_STAMP))[PROBE];
    const unsigned long long now_ = __builtin_amdgcn_s_memrealtime();
    const long long d_ = (long long)(b_ - a_) * PK;
    for (int i_ = 0; i_ < 2000000 && (long long)(__builtin_amdgcn_s_memrealtime() - now_) < d_; ++i_) __builtin_amdgcn_s_sleep(1);
  }
}

extern "C" void kernel_launch(void* const* d_in, const int* in_sizes, int n_in, void* d_out, int out_size, void* d_ws,
                              size_t ws_size, hipStream_t stream) {
  Params p;
  p.x = (const unsigned short*)d_in[0];
  p.k = (const unsigned short*)d_in[1];
  p.v = (const unsigned short*)d_in[2];
  p.wq = (const unsigned short*)d_in[3];
  p.wo = (const unsigned short*)d_in[4];
  p.w1 = (const unsigned short*)d_in[6];
  p.w3 = (const unsigned short*)d_in[7];
  p.w2 = (const unsigned short*)d_in[8];
  p.gw = (const unsigned short*)d_in[9];
  p.out = (unsigned short*)d_out;
  p.ws = (unsigned char*)d_ws;
  hipLaunchKernelGGL(fused_kernel, dim3(NB * NH), dim3(NTHR), 0, stream, p);
}
```

```cpp
#include <hip/hip_runtime.h>
#include <hip/hip_bf16.h>
#include <stdint.h>

typedef unsigned v4u __attribute__((ext_vector_type(4)));
typedef float f32x4 __attribute__((ext_vector_type(4)));
typedef float f32x2 __attribute__((ext_vector_type(2)));
typedef __bf16 bf16x8 __attribute__((ext_vector_type(8)));
typedef __bf16 bf16x2 __attribute__((ext_vector_type(2)));
#define GAS __attribute__((address_space(1)))
#define LAS __attribute__((address_space(3)))
#define DEVI __device__ __forceinline__

#define DEPTH 8
#define HOLD 48
#define RATE 4
#define AUXKV 2
#define AUXW 0
#define T1 3
#define S1 3
#define T2 0
#define S2 5
#define T3 0
#define S3 4
#define NSTAMP 14
#define PROBE0 0
#define PROBE 0
#define PK 0
#define STAMP(i) do { __builtin_amdgcn_sched_barrier(0); const unsigned long long t_ = __builtin_amdgcn_s_memrealtime(); __builtin_amdgcn_s_waitcnt(0xC07F); \
  if (lane == 0 && (w & 3) == 0) ((LAS unsigned long long*)(lds + L_STAMP))[i] = t_; __builtin_amdgcn_sched_barrier(0); } while (0)

constexpr int NB = 8, DM = 512, NH = 32, SK = 4096, DH = 128, WK = 1024, FF = 1376;
constexpr int NCH = FF / 8;
constexpr int NTHR = 512;
constexpr int RSRC_W3 = 0x00020000;
constexpr int AUX_SC1 = 16;
constexpr int OOB = 0x40000000;
constexpr int WS_R = 0, WS_DATA = 256 * 128, WS_SND = WS_DATA + 256 * 4096, WS_END = WS_SND + 256 * 512;
constexpr int SPIN_CAP = 1 << 18;

template <int N> struct IC { static constexpr int value = N; };
template <int LO, int HI, class Fn> DEVI void static_for(Fn&& f) {
  if constexpr (LO < HI) { f(IC<LO>{}); static_for<LO + 1, HI>(f); }
}
DEVI float dot2(unsigned a, unsigned b, float c) {
  return __builtin_amdgcn_fdot2_f32_bf16(__builtin_bit_cast(bf16x2, a), __builtin_bit_cast(bf16x2, b), c, false);
}
DEVI unsigned cvtpk(float lo, float hi) {
  bf16x2 r = {(__bf16)lo, (__bf16)hi};
  return __builtin_bit_cast(unsigned, r);
}
template <int CTRL> DEVI float dppf(float x) {
  return __builtin_bit_cast(float, __builtin_amdgcn_mov_dpp(__builtin_bit_cast(int, x), CTRL, 0xf, 0xf, true));
}
template <int CTRL> DEVI unsigned dppu(unsigned x) { return (unsigned)__builtin_amdgcn_mov_dpp((int)x, CTRL, 0xf, 0xf, true); }
constexpr int XOR1 = 0xB1, XOR2 = 0x4E, XOR7 = 0x141, XOR8 = 0x128, BCAST = 0x150;
DEVI float xrow_max(float x) {
  auto s = __builtin_amdgcn_permlane16_swap(__float_as_uint(x), __float_as_uint(x), false, false);
  x = fmaxf(__uint_as_float(s[0]), __uint_as_float(s[1]));
  auto t = __builtin_amdgcn_permlane32_swap(__float_as_uint(x), __float_as_uint(x), false, false);
  return fmaxf(__uint_as_float(t[0]), __uint_as_float(t[1]));
}
DEVI float xrow_sum(float x) {
  auto s = __builtin_amdgcn_permlane16_swap(__float_as_uint(x), __float_as_uint(x), false, false);
  x = __uint_as_float(s[0]) + __uint_as_float(s[1]);
  auto t = __builtin_amdgcn_permlane32_swap(__float_as_uint(x), __float_as_uint(x), false, false);
  return __uint_as_float(t[0]) + __uint_as_float(t[1]);
}
DEVI float wave_max(float x) {
  x = fmaxf(x, dppf<XOR1>(x)); x = fmaxf(x, dppf<XOR2>(x)); x = fmaxf(x, dppf<XOR7>(x)); x = fmaxf(x, dppf<XOR8>(x));
  return xrow_max(x);
}
DEVI float wave_sum(float x) {
  x += dppf<XOR1>(x); x += dppf<XOR2>(x); x += dppf<XOR7>(x); x += dppf<XOR8>(x);
  return xrow_sum(x);
}
DEVI f32x4 mfma(v4u a, v4u b, f32x4 acc) {
  return __builtin_amdgcn_mfma_f32_16x16x32_bf16(__builtin_bit_cast(bf16x8, a), __builtin_bit_cast(bf16x8, b), acc, 0, 0, 0);
}
template <int T> DEVI v4u pair_a(unsigned word, const v4u& sel) {
  const unsigned wb = dppu<BCAST + T>(word);
  return v4u{__builtin_amdgcn_perm(wb, wb, sel[0]), __builtin_amdgcn_perm(wb, wb, sel[1]),
             __builtin_amdgcn_perm(wb, wb, sel[2]), __builtin_amdgcn_perm(wb, wb, sel[3])};
}
template <int T> DEVI f32x4 rowmfma(unsigned word, const v4u& msk, v4u b, f32x4 acc) {
  v4u a = {dppu<BCAST + T>(word) & msk[0], dppu<BCAST + T>(word) & msk[1], dppu<BCAST + T>(word) & msk[2], dppu<BCAST + T>(word) & msk[3]};
  return mfma(a, b, acc);
}
DEVI void touch(const v4u& x) { asm volatile("" :: "v"(x)); }
DEVI void fence() { __builtin_amdgcn_sched_barrier(0); }
DEVI void wg_barrier() { asm volatile("s_waitcnt lgkmcnt(0)\n\ts_barrier" ::: "memory"); }
DEVI void nap(int n) { for (int i = 0; i < n; ++i) __builtin_amdgcn_s_sleep(8); }
DEVI void lds_arrive(LAS float* p, int lane) {
  asm volatile("" ::: "memory");
  if (lane == 0) __hip_atomic_fetch_add((LAS unsigned*)p, 1u, __ATOMIC_RELAXED, __HIP_MEMORY_SCOPE_WORKGROUP);
}
DEVI void lds_wait4(LAS float* p) {
  while (__hip_atomic_load((LAS unsigned*)p, __ATOMIC_RELAXED, __HIP_MEMORY_SCOPE_WORKGROUP) < 4u) __builtin_amdgcn_s_sleep(1);
  asm volatile("" ::: "memory");
}

struct Params {
  const unsigned short *x, *k, *v, *wq, *wo, *w1, *w3, *w2, *gw;
  unsigned short* out;
  unsigned char* ws;
};

constexpr int L_Q = 0;
constexpr int L_GU = L_Q + 4 * 128;
constexpr int L_O = L_GU + 4 * 256;
constexpr int L_ST = L_O + 4 * 128;
constexpr int L_USED = L_ST + 8;
constexpr int L_NEW = L_USED + 32;
constexpr int L_CNT = L_NEW + 32;
constexpr int L_STAMP = L_CNT + 4;
constexpr int L_END = L_STAMP + 2 * 16;

__global__ void __launch_bounds__(NTHR, 2) __attribute__((amdgpu_waves_per_eu(2, 2))) fused_kernel(Params p) {
  __shared__ __attribute__((aligned(16))) float lds_[L_END];
  LAS float* lds = (LAS float*)lds_;
  unsigned long long tentry;
  asm volatile("s_memrealtime %0\n\ts_waitcnt lgkmcnt(0)" : "=s"(tentry) :: "memory");
  __builtin_amdgcn_sched_barrier(0);
  const int tid = threadIdx.x, lane = tid & 63, w = __builtin_amdgcn_readfirstlane(tid >> 6);
  const int r = lane >> 4, c = lane & 15;
  const int bid = blockIdx.x, h = bid & 31, b = bid >> 5;
  const int hp = (h & 7) * 4 + (h >> 3);
  const __amdgpu_buffer_rsrc_t rw = __builtin_amdgcn_make_buffer_rsrc((void*)p.ws, 0, WS_END, RSRC_W3);
  v4u msk;
#pragma unroll
  for (int d = 0; d < 4; ++d) msk[d] = ((c >> 1) == d) ? ((c & 1) ? 0xffff0000u : 0x0000ffffu) : 0u;

  if (w < 4) {
    STAMP(0);
    if (lane == 0 && w == 0) ((LAS unsigned long long*)(lds + L_STAMP))[12] = tentry;
    const size_t kvo = ((size_t)bid * SK + (SK - WK)) * DH;
    const __amdgpu_buffer_rsrc_t rk = __builtin_amdgcn_make_buffer_rsrc((void*)(p.k + kvo), 0, WK * DH * 2, RSRC_W3);
    const __amdgpu_buffer_rsrc_t rv = __builtin_amdgcn_make_buffer_rsrc((void*)(p.v + kvo), 0, WK * DH * 2, RSRC_W3);
    const int vkv = w * 4096 + lane * 16;
    v4u kv[128];
    auto issue = [&](auto N) {
      constexpr int n = N.value;
      if constexpr (n < 128) kv[n] = __builtin_amdgcn_raw_buffer_load_b128(n < 64 ? rk : rv, vkv + (n & 3) * 1024, ((n >> 2) & 15) * 16384, AUXKV);
      fence();
    };
    static_for<0, DEPTH>([&](auto N) { issue(N); });
    touch(kv[0]); STAMP(11);
    static_for<DEPTH, HOLD>([&](auto N) { touch(kv[N.value - DEPTH]); fence(); issue(N); });
    STAMP(1);
    wg_barrier();
    STAMP(2);
    v4u qv;
    {
      f32x4 q0 = *(const LAS f32x4*)&lds[L_Q + 8 * c], q1 = *(const LAS f32x4*)&lds[L_Q + 8 * c + 4];
#pragma unroll
      for (int u = 1; u < 4; ++u) { q0 += *(const LAS f32x4*)&lds[L_Q + u * 128 + 8 * c]; q1 += *(const LAS f32x4*)&lds[L_Q + u * 128 + 8 * c + 4]; }
      qv = v4u{cvtpk(q0[0], q0[1]), cvtpk(q0[2], q0[3]), cvtpk(q1[0], q1[1]), cvtpk(q1[2], q1[3])};
    }
    float xs[16], sc[4];
    unsigned pw[4];
    f32x4 ao = {0.f, 0.f, 0.f, 0.f};
    auto consume = [&](auto I) {
      constexpr int i = I.value;
      if constexpr (i < 64) {
        float d = dot2(kv[i][0], qv[0], 0.f);
        d = dot2(kv[i][1], qv[1], d); d = dot2(kv[i][2], qv[2], d); d = dot2(kv[i][3], qv[3], d);
        xs[i & 15] = d;
        if constexpr ((i & 15) == 15) {
#define TR_STEP(HALF, CTRL) _Pragma("unroll") for (int u = 0; u < (HALF); ++u) { \
          const float lo_ = xs[u], hi_ = xs[u + (HALF)]; const float t1_ = lo_ + dppf<CTRL>(lo_), t2_ = hi_ + dppf<CTRL>(hi_); \
          xs[u] = (c & (HALF)) ? t2_ : t1_; }
          TR_STEP(8, XOR8) TR_STEP(4, XOR7) TR_STEP(2, XOR2) TR_STEP(1, XOR1)
#undef TR_STEP
          sc[i >> 4] = xs[0] * (0.08838834764831845f * 1.4426950408889634f);
        }
        if constexpr (i == 63) {
          const float mw = wave_max(fmaxf(fmaxf(sc[0], sc[1]), fmaxf(sc[2], sc[3])));
          float e[4];
#pragma unroll
          for (int u = 0; u < 4; ++u) { e[u] = __builtin_amdgcn_exp2f(sc[u] - mw); pw[u] = cvtpk(e[u], e[u]); }
          const float lw = wave_sum((e[0] + e[1]) + (e[2] + e[3]));
          if (lane == 0) *(LAS f32x2*)&lds[L_ST + 2 * w] = f32x2{mw, lw};
          lds_arrive(lds + L_CNT + 1, lane);
          STAMP(3);
        }
      } else {
        ao = rowmfma<(i & 15)>(pw[(i - 64) >> 4], msk, kv[i], ao);
      }
      fence();
    };
    static_for<HOLD, 128 + DEPTH>([&](auto N) {
      constexpr int n = N.value;
      constexpr int c0 = (n - HOLD) * RATE < n - DEPTH ? (n - HOLD) * RATE : n - DEPTH;
      constexpr int c1 = (n + 1 - HOLD) * RATE < n + 1 - DEPTH ? (n + 1 - HOLD) * RATE : n + 1 - DEPTH;
      static_for<c0, c1>([&](auto I) { consume(I); });
      if constexpr (c1 <= n - DEPTH) { touch(kv[n - DEPTH]); fence(); }
      issue(N);
    });
    STAMP(4);
    if (r < 2) *(LAS f32x4*)&lds[L_O + w * 128 + 8 * c + 4 * r] = ao;
    wg_barrier();
    if (w == 0) {
      const unsigned nonce = __float_as_uint(lds[L_CNT + 2]);
      const int slot = b * 32 + hp;
      const int od = WS_DATA + slot * 4096 + lane * 16, os = lane < 32 ? WS_SND + slot * 512 + lane * 16 : OOB;
      v4u d[4], s;
      bool ok[4] = {false, false, false, false}, oks = lane >= 32;
      for (int spin = 0; spin < SPIN_CAP; ++spin) {
#pragma unroll
        for (int q = 0; q < 4; ++q) if (!ok[q]) d[q] = __builtin_amdgcn_raw_buffer_load_b128(rw, od + q * 1024, 0, AUX_SC1);
        if (!oks) s = __builtin_amdgcn_raw_buffer_load_b128(rw, os, 0, AUX_SC1);
#pragma unroll
        for (int q = 0; q < 4; ++q) ok[q] = d[q][1] == nonce && d[q][3] == nonce;
        oks = lane >= 32 || s[1] == nonce;
        if (__all(ok[0] && ok[1] && ok[2] && ok[3] && oks)) break;
      }
      float s0 = (__uint_as_float(d[0][0]) + __uint_as_float(d[1][0])) + (__uint_as_float(d[2][0]) + __uint_as_float(d[3][0]));
      float s1 = (__uint_as_float(d[0][2]) + __uint_as_float(d[1][2])) + (__uint_as_float(d[2][2]) + __uint_as_float(d[3][2]));
      s0 += dppf<XOR8>(s0); s1 += dppf<XOR8>(s1);
      s0 = xrow_sum(s0); s1 = xrow_sum(s1);
      if (lane < 8) *(GAS unsigned*)((GAS unsigned short*)p.out + b * DM + hp * 16 + 2 * lane) = cvtpk(s0, s1);
      STAMP(5);
      asm volatile("s_waitcnt vmcnt(0)" ::: "memory");
      STAMP(13);
      const int g2 = (lane & 7) * 4 + ((lane >> 3) & 3);
      bool stale = false;
      if (lane < 32) { stale = __float_as_uint(lds[L_USED + g2]) != s[0]; lds[L_NEW + g2] = __uint_as_float(s[0]); }
      const bool any = __any(stale);
      if (lane == 0) lds[L_CNT + 3] = __uint_as_float(any ? 1u : 0u);
    }
    wg_barrier();
  } else {
    __builtin_amdgcn_s_setprio(3);
    const int ww = w - 4, j = h >> 3;
    const int ch0 = (NCH * hp) >> 5, nch = ((NCH * (hp + 1)) >> 5) - ch0;
    const __amdgpu_buffer_rsrc_t rq = __builtin_amdgcn_make_buffer_rsrc((void*)(p.wq), 0, DM * NH * DH * 2, RSRC_W3);
    const int vq = (128 * ww + r) * 8192 + h * 256 + c * 16;
    const GAS unsigned short* xb = (const GAS unsigned short*)p.x + b * DM;
    const unsigned xq0 = xb[128 * ww + 4 * ((c + 4 * b) & 31) + r], xq1 = xb[128 * ww + 4 * ((16 + c + 4 * b) & 31) + r];
    v4u wq[32], w1[16], w3[16], wo[32], w2[12];
    auto ldq = [&](auto I) { wq[I.value] = __builtin_amdgcn_raw_buffer_load_b128(rq, vq, ((I.value + 4 * b) & 31) * 32768, AUXW); fence(); };
    static_for<0, 4>(ldq);
    const __amdgpu_buffer_rsrc_t r1 = __builtin_amdgcn_make_buffer_rsrc((void*)(p.w1), 0, DM * FF * 2, RSRC_W3);
    const __amdgpu_buffer_rsrc_t r3 = __builtin_amdgcn_make_buffer_rsrc((void*)(p.w3), 0, DM * FF * 2, RSRC_W3);
    const __amdgpu_buffer_rsrc_t ro = __builtin_amdgcn_make_buffer_rsrc((void*)(p.wo + (size_t)h * DH * DM), 0, DH * DM * 2, RSRC_W3);
    const __amdgpu_buffer_rsrc_t r2 = __builtin_amdgcn_make_buffer_rsrc((void*)(p.w2 + (size_t)ch0 * 8 * DM), 0, nch * 8 * DM * 2, RSRC_W3);
    const int v13 = ((c & 7) < nch) ? (128 * ww + 2 * r + (c >> 3)) * (FF * 2) + (ch0 + (c & 7)) * 16 : OOB;
    const int vo = r * 1024 + ww * 256 + c * 16;
    const int rot2 = (3 * b) >> 1;
    auto ld13 = [&](auto I) {
      w1[I.value] = __builtin_amdgcn_raw_buffer_load_b128(r1, v13, ((I.value + 2 * b) & 15) * (8 * FF * 2), AUXW);
      w3[I.value] = __builtin_amdgcn_raw_buffer_load_b128(r3, v13, ((I.value + 2 * b) & 15) * (8 * FF * 2), AUXW);
      fence();
    };
    auto ldo = [&](auto I) { wo[I.value] = __builtin_amdgcn_raw_buffer_load_b128(ro, vo, ((I.value + 4 * b) & 31) * 4096, AUXW); fence(); };
    auto ld2 = [&](auto I) { const int s = I.value + rot2; w2[I.value] = __builtin_amdgcn_raw_buffer_load_b128(r2, vo, (s >= 12 ? s - 12 : s) * 4096, AUXW); fence(); };
    const unsigned xp = *(const GAS unsigned*)(xb + 128 * ww + 8 * ((c + 2 * b) & 15) + 2 * r);
    const v4u xg = *(const GAS v4u*)(xb + 8 * lane);
    const v4u gw = *(const GAS v4u*)((const GAS unsigned short*)p.gw + 8 * lane);
    fence();
    static_for<0, 2>(ld13);
    static_for<0, 2>(ld2);
    static_for<0, 4>(ldo);
    if (ww == 3) {
      const int slot = b * 32 + hp;
      const int od = WS_DATA + slot * 4096 + lane * 16, os = lane < 32 ? WS_SND + slot * 512 + lane * 16 : OOB;
      const unsigned old = __builtin_amdgcn_raw_buffer_load_b32(rw, 0, WS_R + slot * 128, AUX_SC1);
      v4u d[5];
#pragma unroll
      for (int q = 0; q < 4; ++q) d[q] = __builtin_amdgcn_raw_buffer_load_b128(rw, od + q * 1024, 0, AUX_SC1);
      d[4] = __builtin_amdgcn_raw_buffer_load_b128(rw, os, 0, AUX_SC1);
      unsigned nonce = __builtin_amdgcn_readfirstlane(old);
      for (;;) {
        ++nonce;
        bool hit = false;
#pragma unroll
        for (int q = 0; q < 5; ++q) hit |= d[q][1] == nonce || d[q][3] == nonce;
        if (!__any(hit)) break;
      }
      if (lane == 0) {
        __builtin_amdgcn_raw_buffer_store_b32(nonce, rw, 0, WS_R + slot * 128, AUX_SC1);
        lds[L_CNT + 0] = 0.f; lds[L_CNT + 1] = 0.f; lds[L_CNT + 2] = __uint_as_float(nonce);
      }
      asm volatile("s_waitcnt vmcnt(0)" ::: "memory");
    }
    v4u sel;
    {
      const unsigned hb = (c >> 3) ? 0x0302u : 0x0100u;
      const unsigned s = (c & 1) ? ((hb << 16) | 0x0c0cu) : (0x0c0c0000u | hb);
#pragma unroll
      for (int d = 0; d < 4; ++d) sel[d] = (((c & 7) >> 1) == d) ? s : 0x0c0c0c0cu;
    }
    const unsigned xq[2] = {xq0 | (xq0 << 16), xq1 | (xq1 << 16)};
    f32x4 aq = {0.f, 0.f, 0.f, 0.f}, ag = aq, au = aq, af = aq;
    auto useq = [&](auto I) { aq = rowmfma<(I.value & 15)>(xq[I.value >> 4], msk, wq[I.value], aq); fence(); };
    auto use13 = [&](auto I) { const v4u a = pair_a<I.value>(xp, sel); ag = mfma(a, w1[I.value], ag); au = mfma(a, w3[I.value], au); fence(); };
    nap(T1 + S1 * j);
    fence();
    static_for<4, 32>(ldq);
    static_for<0, 4>(useq);
    static_for<0, 2>(use13);
    float g;
    {
      float d = dot2(xg[0], gw[0], 0.f);
      d = dot2(xg[1], gw[1], d); d = dot2(xg[2], gw[2], d); d = dot2(xg[3], gw[3], d);
      d = wave_sum(d);
      g = 1.f / (1.f + __expf(-d));
      asm volatile("" : "+v"(g));
    }
    static_for<4, 32>(useq);
    if (r < 2) *(LAS f32x4*)&lds[L_Q + ww * 128 + 8 * c + 4 * r] = aq;
    STAMP(6);
    wg_barrier();
    nap(T2 + S2 * j);
    fence();
    static_for<2, 16>(ld13);
    static_for<2, 16>(use13);
    if ((r >> 1) == (c >> 3)) {
      *(LAS f32x4*)&lds[L_GU + ww * 256 + (r >> 1) * 128 + 8 * (c & 7) + 4 * (r & 1)] = ag;
      *(LAS f32x4*)&lds[L_GU + ww * 256 + (r >> 1) * 128 + 64 + 8 * (c & 7) + 4 * (r & 1)] = au;
    }
    lds_arrive(lds + L_CNT + 0, lane);
    lds_wait4(lds + L_CNT + 0);
    STAMP(7);
    unsigned hw;
    {
      const int s2 = c + rot2 >= 12 ? c + rot2 - 12 : c + rot2;
      const int f = (4 * s2 + r) & 63;
      float gt = 0.f, up = 0.f;
#pragma unroll
      for (int u = 0; u < 8; ++u) { gt += lds[L_GU + u * 128 + f]; up += lds[L_GU + u * 128 + 64 + f]; }
      const float hv = gt / (1.f + __expf(-gt)) * up;
      hw = (c < 12 && f < 8 * nch) ? cvtpk(hv, hv) : 0u;
    }
    lds_wait4(lds + L_CNT + 1);
    float fw[4];
    {
      f32x2 st[4];
#pragma unroll
      for (int u = 0; u < 4; ++u) st[u] = *(const LAS f32x2*)&lds[L_ST + 2 * u];
      const float M = fmaxf(fmaxf(st[0][0], st[1][0]), fmaxf(st[2][0], st[3][0]));
      float L = 0.f;
#pragma unroll
      for (int u = 0; u < 4; ++u) { fw[u] = __builtin_amdgcn_exp2f(st[u][0] - M); L += fw[u] * st[u][1]; }
      const float inv = g / L;
#pragma unroll
      for (int u = 0; u < 4; ++u) { fw[u] *= inv; asm volatile("" : "+v"(fw[u])); }
    }
    nap(T3 + S3 * j);
    fence();
    static_for<2, 12>(ld2);
    static_for<4, 32>(ldo);
    const int cg = 8 * ww + (c >> 1);
    unsigned tag = __builtin_amdgcn_raw_buffer_load_b32(rw, WS_R + (b * 32 + cg) * 128, 0, AUX_SC1);
    fence();
    static_for<0, 12>([&](auto I) { af = rowmfma<I.value>(hw, msk, w2[I.value], af); fence(); });
    af *= (1.f - g);
    if (r == 3 && (c & 1) == 0) lds[L_USED + cg] = __uint_as_float(tag);
    const unsigned mine = __float_as_uint(lds[L_CNT + 2]);
    const int slot = b * 32 + cg;
    const int o1 = r < 2 ? WS_DATA + slot * 4096 + h * 128 + (8 * (c & 1) + 4 * r) * 8 : (r == 2 && (c & 1) == 0) ? WS_SND + slot * 512 + h * 16 : OOB;
    const int o2 = r < 2 ? o1 + 16 : OOB;
    const int lo0 = L_O + 4 * ((c + 4 * b) & 31) + r, lo1 = L_O + 4 * ((16 + c + 4 * b) & 31) + r;
    static_for<0, 32>([&](auto I) { touch(wo[I.value]); });
    asm volatile("" : "+v"(af));
    STAMP(8);
    wg_barrier();
    STAMP(9);
    float o0 = lds[lo0] * fw[0], o1v = lds[lo1] * fw[0];
#pragma unroll
    for (int u = 1; u < 4; ++u) { o0 = fmaf(lds[lo0 + u * 128], fw[u], o0); o1v = fmaf(lds[lo1 + u * 128], fw[u], o1v); }
    const unsigned ow[2] = {cvtpk(o0, o0), cvtpk(o1v, o1v)};
    static_for<0, 32>([&](auto I) { af = rowmfma<(I.value & 15)>(ow[I.value >> 4], msk, wo[I.value], af); });
    v4u m1 = {r < 2 ? __float_as_uint(af[0]) : mine, tag, r < 2 ? __float_as_uint(af[1]) : mine, tag};
    v4u m2 = {__float_as_uint(af[2]), tag, __float_as_uint(af[3]), tag};
    __builtin_amdgcn_raw_buffer_store_b128(m1, rw, o1, 0, AUX_SC1);
    __builtin_amdgcn_raw_buffer_store_b128(m2, rw, o2, 0, AUX_SC1);
    STAMP(10);
    wg_barrier();
    if (__float_as_uint(lds[L_CNT + 3]) != 0u) {
      tag = __float_as_uint(lds[L_NEW + cg]);
      m1[1] = tag; m1[3] = tag; m2[1] = tag; m2[3] = tag;
      __builtin_amdgcn_raw_buffer_store_b128(m1, rw, o1, 0, AUX_SC1);
      __builtin_amdgcn_raw_buffer_store_b128(m2, rw, o2, 0, AUX_SC1);
    }
  }
  if (w == 0) {
    const unsigned long long a_ = ((LAS unsigned long long*)(lds + L_STAMP))[PROBE0], b_ = ((LAS unsigned long long*)(lds + L_STAMP))[PROBE];
    const unsigned long long now_ = __builtin_amdgcn_s_memrealtime();
    const long long d_ = (long long)(b_ - a_) * PK;
    for (int i_ = 0; i_ < 2000000 && (long long)(__builtin_amdgcn_s_memrealtime() - now_) < d_; ++i_) __builtin_amdgcn_s_sleep(1);
  }
}

extern "C" void kernel_launch(void* const* d_in, const int* in_sizes, int n_in, void* d_out, int out_size, void* d_ws,
                              size_t ws_size, hipStream_t stream) {
  Params p;
  p.x = (const unsigned short*)d_in[0];
  p.k = (const unsigned short*)d_in[1];
  p.v = (const unsigned short*)d_in[2];
  p.wq = (const unsigned short*)d_in[3];
  p.wo = (const unsigned short*)d_in[4];
  p.w1 = (const unsigned short*)d_in[6];
  p.w3 = (const unsigned short*)d_in[7];
  p.w2 = (const unsigned short*)d_in[8];
  p.gw = (const unsigned short*)d_in[9];
  p.out = (unsigned short*)d_out;
  p.ws = (unsigned char*)d_ws;
  hipLaunchKernelGGL(fused_kernel, dim3(NB * NH), dim3(NTHR), 0, stream, p);
}
```

```cpp
#include <hip/hip_runtime.h>
#include <hip/hip_bf16.h>
#include <stdint.h>

typedef unsigned v4u __attribute__((ext_vector_type(4)));
typedef unsigned v8u __attribute__((ext_vector_type(8)));
typedef float f32x4 __attribute__((ext_vector_type(4)));
typedef float f32x2 __attribute__((ext_vector_type(2)));
typedef __bf16 bf16x8 __attribute__((ext_vector_type(8)));
typedef __bf16 bf16x16 __attribute__((ext_vector_type(16)));
typedef __bf16 bf16x2 __attribute__((ext_vector_type(2)));
#define GAS __attribute__((address_space(1)))
#define LAS __attribute__((address_space(3)))
#define DEVI __device__ __forceinline__

#define DEPTH 10
#define HOLD 50
#define RATE 4
#define AUXKV 2
#define AUXW 0
#define T1 230
#define S1 90
#define T2 650
#define S2 140
#define T3 1200
#define S3 130
#define DLOW 5
#define DELTA 20
#define CFROM 72
#define PUBBAR 1
#define PD0 18
#define PB 0
#define MANY 64
#define NPOLL 1
#define PSTAG 5
#define AUXOUT 0
#define PRIO0 1
#define CODEPF 0
#define NSTAMP 0
#define STAMP(i)
#define STAMPW(i)

constexpr int NB = 8, DM = 512, NH = 32, SK = 4096, DH = 128, WK = 1024, FF = 1376;
constexpr int NCH = FF / 8;
constexpr int NTHR = 512;
constexpr int RSRC_W3 = 0x00020000;
constexpr int AUX_SC1 = 16;
constexpr int OOB = 0x40000000;
constexpr int WS_R = 0, WS_DATA = 256 * 128, WS_END = WS_DATA + 256 * 4096;
constexpr int SPIN_CAP = 1 << 18;

template <int N> struct IC { static constexpr int value = N; };
template <int LO, int HI, class Fn> DEVI void static_for(Fn&& f) {
  if constexpr (LO < HI) { f(IC<LO>{}); static_for<LO + 1, HI>(f); }
}
DEVI float dot2(unsigned a, unsigned b, float c) {
  return __builtin_amdgcn_fdot2_f32_bf16(__builtin_bit_cast(bf16x2, a), __builtin_bit_cast(bf16x2, b), c, false);
}
DEVI unsigned cvtpk(float lo, float hi) {
  bf16x2 r = {(__bf16)lo, (__bf16)hi};
  return __builtin_bit_cast(unsigned, r);
}
template <int CTRL> DEVI float dppf(float x) {
  return __builtin_bit_cast(float, __builtin_amdgcn_mov_dpp(__builtin_bit_cast(int, x), CTRL, 0xf, 0xf, true));
}
template <int CTRL> DEVI unsigned dppu(unsigned x) { return (unsigned)__builtin_amdgcn_mov_dpp((int)x, CTRL, 0xf, 0xf, true); }
constexpr int XOR1 = 0xB1, XOR2 = 0x4E, XOR7 = 0x141, XOR8 = 0x128, BCAST = 0x150;
DEVI float xrow_max(float x) {
  auto s = __builtin_amdgcn_permlane16_swap(__float_as_uint(x), __float_as_uint(x), false, false);
  x = fmaxf(__uint_as_float(s[0]), __uint_as_float(s[1]));
  auto t = __builtin_amdgcn_permlane32_swap(__float_as_uint(x), __float_as_uint(x), false, false);
  return fmaxf(__uint_as_float(t[0]), __uint_as_float(t[1]));
}
DEVI float xrow_sum(float x) {
  auto s = __builtin_amdgcn_permlane16_swap(__float_as_uint(x), __float_as_uint(x), false, false);
  x = __uint_as_float(s[0]) + __uint_as_float(s[1]);
  auto t = __builtin_amdgcn_permlane32_swap(__float_as_uint(x), __float_as_uint(x), false, false);
  return __uint_as_float(t[0]) + __uint_as_float(t[1]);
}
DEVI float wave_max(float x) {
  x = fmaxf(x, dppf<XOR1>(x)); x = fmaxf(x, dppf<XOR2>(x)); x = fmaxf(x, dppf<XOR7>(x)); x = fmaxf(x, dppf<XOR8>(x));
  return xrow_max(x);
}
DEVI float wave_sum(float x) {
  x += dppf<XOR1>(x); x += dppf<XOR2>(x); x += dppf<XOR7>(x); x += dppf<XOR8>(x);
  return xrow_sum(x);
}
DEVI f32x4 mfma(v4u a, v4u b, f32x4 acc) {
  return __builtin_amdgcn_mfma_f32_16x16x32_bf16(__builtin_bit_cast(bf16x8, a), __builtin_bit_cast(bf16x8, b), acc, 0, 0, 0);
}
template <int T> DEVI v4u pair_a(unsigned word, const v4u& sel) {
  const unsigned wb = dppu<BCAST + T>(word);
  return v4u{__builtin_amdgcn_perm(wb, wb, sel[0]), __builtin_amdgcn_perm(wb, wb, sel[1]),
             __builtin_amdgcn_perm(wb, wb, sel[2]), __builtin_amdgcn_perm(wb, wb, sel[3])};
}
struct RowSel { unsigned m0, m1; int idx; };
template <int T> DEVI f32x4 rowsmf(unsigned word, const RowSel& q, v4u b0, v4u b1, f32x4 acc) {
  const v4u a = {dppu<BCAST + T>(word) & q.m0, dppu<BCAST + T>(word) & q.m1, dppu<BCAST + T + 1>(word) & q.m0, dppu<BCAST + T + 1>(word) & q.m1};
  const v8u b = __builtin_shufflevector(b0, b1, 0, 1, 2, 3, 4, 5, 6, 7);
  return __builtin_amdgcn_smfmac_f32_16x16x64_bf16(__builtin_bit_cast(bf16x8, a), __builtin_bit_cast(bf16x16, b), acc, q.idx, 0, 0);
}
DEVI void touch(const v4u& x) { asm volatile("" :: "v"(x)); }
DEVI void fence() { __builtin_amdgcn_sched_barrier(0); }
DEVI void wg_barrier() { asm volatile("s_waitcnt lgkmcnt(0)\n\ts_barrier" ::: "memory"); }
DEVI void pace(unsigned long long t0, int ticks) {
  for (int i = 0; i < 4096 && (int)(__builtin_amdgcn_s_memrealtime() - t0) < ticks; ++i) __builtin_amdgcn_s_sleep(2);
}
DEVI void lds_arrive(LAS float* p, int lane) {
  asm volatile("" ::: "memory");
  if (lane == 0) __hip_atomic_fetch_add((LAS unsigned*)p, 1u, __ATOMIC_RELAXED, __HIP_MEMORY_SCOPE_WORKGROUP);
}
DEVI void lds_wait4(LAS float* p) {
  while (__hip_atomic_load((LAS unsigned*)p, __ATOMIC_RELAXED, __HIP_MEMORY_SCOPE_WORKGROUP) < 4u) __builtin_amdgcn_s_sleep(1);
  asm volatile("" ::: "memory");
}

#define WCMP(a, b, p) "v_cmp_ne_u32_sdwa %[ta], " #a ", %[nn] src0_sel:WORD_1 src1_sel:DWORD\n\tv_cmp_ne_u32_sdwa %[tb], " #b ", %[nn] src0_sel:WORD_1 src1_sel:DWORD\n\ts_or_b64 %[" #p "], %[ta], %[tb]\n\t"
constexpr unsigned long long sched4(int a, int b, int c, int d) {
  return (unsigned long long)(a + DELTA) | (unsigned long long)(b + DELTA) << 16 | (unsigned long long)(c + DELTA) << 32 | (unsigned long long)(d + DELTA) << 48;
}
constexpr unsigned long long SCHED[13] = {
    sched4(564, 529, 375, 374), sched4(775, 640, 610, 450), sched4(862, 728, 743, 653), sched4(964, 929, 840, 824), sched4(1045, 1023, 932, 925),
    sched4(1096, 1087, 1121, 969), sched4(1374, 1225, 1250, 1219), sched4(1448, 1301, 1325, 1308), sched4(1526, 1496, 1410, 1403),
    sched4(1612, 1590, 1621, 1485), sched4(1695, 1690, 1705, 1563), sched4(1782, 1780, 1786, 1780), sched4(1880, 1880, 1880, 1880)};
struct Params {
  const unsigned short *x, *k, *v, *wq, *wo, *w1, *w3, *w2, *gw;
  unsigned short* out;
  unsigned char* ws;
};

constexpr int L_Q = 0;
constexpr int L_GU = L_Q + 4 * 128;
constexpr int L_O = L_GU + 4 * 256;
constexpr int L_ST = L_O + 4 * 128;
constexpr int L_USED = L_ST + 8;
constexpr int L_NEW = L_USED + 32;
constexpr int L_CNT = L_NEW + 32;
constexpr int L_STAMP = L_CNT + 8;
constexpr int L_END = L_STAMP + 2 * 32;

__global__ void __launch_bounds__(NTHR, 2) __attribute__((amdgpu_waves_per_eu(2, 2))) fused_kernel(Params p) {
  __shared__ __attribute__((aligned(16))) float lds_[L_END];
  LAS float* lds = (LAS float*)lds_;
  const int tid = threadIdx.x, lane = tid & 63, w = __builtin_amdgcn_readfirstlane(tid >> 6);
  const int r = lane >> 4, c = lane & 15;
  const int bid = blockIdx.x, h = bid & 31, b = bid >> 5;
  const int hp = (h & 7) * 4 + (h >> 3);
  RowSel rs;
  {
    const unsigned hm = (c & 3) == 3 ? 0xffff0000u : 0x0000ffffu;
    rs.m0 = (c & 4) ? 0u : hm; rs.m1 = (c & 4) ? hm : 0u;
    rs.idx = (((c & 3) == 3 ? 0 : (c & 3)) | 12) * 0x1111;
  }
  const int rp = (r >> 1) + 4 * (r & 1);
#define USE_ALL_ARGS() asm volatile("" :: "s"(p.x), "s"(p.k), "s"(p.v), "s"(p.wq), "s"(p.wo), "s"(p.w1), "s"(p.w3), "s"(p.w2), "s"(p.gw), "s"(p.out), "s"(p.ws))

  if (__builtin_expect(w < 4, 1)) {
    USE_ALL_ARGS();
    STAMP(0);
    const size_t kvo = ((size_t)bid * SK + (SK - WK)) * DH;
    const __amdgpu_buffer_rsrc_t rk = __builtin_amdgcn_make_buffer_rsrc((void*)(p.k + kvo), 0, WK * DH * 2, RSRC_W3);
    const __amdgpu_buffer_rsrc_t rv = __builtin_amdgcn_make_buffer_rsrc((void*)(p.v + kvo), 0, WK * DH * 2, RSRC_W3);
    const int vk = w * 4096 + lane * 16, vv = w * 4096 + rp * 256 + c * 16;
    v4u kv[128];
    const unsigned ts0 = (unsigned)__builtin_amdgcn_s_memrealtime();
    unsigned tnow = 0, tel = 1, tth = 0;
    auto issue = [&](auto N) {
      constexpr int n = N.value;
      if constexpr (n >= CFROM && n < 128) {
        if constexpr ((n & 7) == 0) tnow = (unsigned)__builtin_amdgcn_s_memrealtime();
        if constexpr ((n & 7) == 1) { tel = tnow - ts0; tth = (unsigned)(SCHED[n / 8 - 3] >> (16 * (h >> 3))) & 0xffffu; }
        if constexpr (n > CFROM) asm volatile("s_cmp_lt_u32 %0, %1\n\ts_cbranch_scc0 1f\n\ts_waitcnt vmcnt(%2)\n1:" :: "s"(tel), "s"(tth), "i"(DLOW - 1) : "scc");
        fence();
      }
      if constexpr (n < 64) kv[n] = __builtin_amdgcn_raw_buffer_load_b128(rk, vk + (n & 3) * 1024, (n >> 2) * 16384, AUXKV);
      else if constexpr (n < 128) kv[n] = __builtin_amdgcn_raw_buffer_load_b128(rv, vv + (n & 1) * 512 + (n & 2) * 1024, ((n >> 2) & 15) * 16384, AUXKV);
      fence();
    };
    static_for<0, DEPTH>([&](auto N) { issue(N); });
    wg_barrier();
    static_for<DEPTH, HOLD>([&](auto N) { touch(kv[N.value - DEPTH]); fence(); issue(N); });
    STAMP(1);
    lds_wait4(lds + L_CNT + 3);
    STAMP(2);
    v4u qv;
    {
      f32x4 q0 = *(const LAS f32x4*)&lds[L_Q + 8 * c], q1 = *(const LAS f32x4*)&lds[L_Q + 8 * c + 4];
#pragma unroll
      for (int u = 1; u < 4; ++u) { q0 += *(const LAS f32x4*)&lds[L_Q + u * 128 + 8 * c]; q1 += *(const LAS f32x4*)&lds[L_Q + u * 128 + 8 * c + 4]; }
      qv = v4u{cvtpk(q0[0], q0[1]), cvtpk(q0[2], q0[3]), cvtpk(q1[0], q1[1]), cvtpk(q1[2], q1[3])};
    }
    int od = WS_DATA + (b * 32 + hp) * 4096 + lane * 16, lw = (L_O + w * 128 + 8 * c + 4 * (r & 1)) * 4, fa = (int)(size_t)(lds + L_CNT + 5);
    int oo = (b * DM + 32 * (hp >> 1) + 4 * (hp & 1) + 8 * (lane & 3) + 2 * ((lane >> 2) & 1)) * 2;
    asm volatile("" : "+v"(od), "+v"(lw), "+v"(fa), "+v"(oo));
    const __amdgpu_buffer_rsrc_t rout = __builtin_amdgcn_make_buffer_rsrc((void*)p.out, 0, NB * DM * 2, RSRC_W3);
    unsigned nonce;
    float xs[16], sc[4];
    unsigned pw[4];
    f32x4 ao = {0.f, 0.f, 0.f, 0.f};
    auto consume = [&](auto I) {
      constexpr int i = I.value;
      if constexpr (i < 64) {
        float d = dot2(kv[i][0], qv[0], 0.f);
        d = dot2(kv[i][1], qv[1], d); d = dot2(kv[i][2], qv[2], d); d = dot2(kv[i][3], qv[3], d);
        xs[i & 15] = d;
        if constexpr ((i & 15) == 15) {
#define TR_STEP(HALF, CTRL) _Pragma("unroll") for (int u = 0; u < (HALF); ++u) { \
          const float lo_ = xs[u], hi_ = xs[u + (HALF)]; const float t1_ = lo_ + dppf<CTRL>(lo_), t2_ = hi_ + dppf<CTRL>(hi_); \
          xs[u] = (c & (HALF)) ? t2_ : t1_; }
          TR_STEP(8, XOR8) TR_STEP(4, XOR7) TR_STEP(2, XOR2) TR_STEP(1, XOR1)
#undef TR_STEP
          sc[i >> 4] = xs[0] * (0.08838834764831845f * 1.4426950408889634f);
        }
        if constexpr (i == 63) {
          const float mw = wave_max(fmaxf(fmaxf(sc[0], sc[1]), fmaxf(sc[2], sc[3])));
          float e[4];
#pragma unroll
          for (int u = 0; u < 4; ++u) { e[u] = __builtin_amdgcn_exp2f(sc[u] - mw); pw[u] = cvtpk(e[u], e[u]); }
          const float lw = wave_sum((e[0] + e[1]) + (e[2] + e[3]));
          if (lane == 0) *(LAS f32x2*)&lds[L_ST + 2 * w] = f32x2{mw, lw};
          lds_arrive(lds + L_CNT + 1, lane);
          STAMP(3);
        }
      } else {
        if constexpr (i == 100) { lds_wait4(lds + L_CNT + 0); nonce = __float_as_uint(lds[L_CNT + 2]); asm volatile("" : "+v"(nonce)); }
        if constexpr (i & 1) ao = rowsmf<((i - 1) & 15)>(pw[(i - 64) >> 4], rs, kv[i - 1], kv[i], ao);
        else touch(kv[i]);
      }
      fence();
    };
    static_for<HOLD, 128 + DEPTH>([&](auto N) {
      constexpr int n = N.value;
      constexpr int c0 = (n - HOLD) * RATE < n - DEPTH ? (n - HOLD) * RATE : n - DEPTH;
      constexpr int c1 = (n + 1 - HOLD) * RATE < n + 1 - DEPTH ? (n + 1 - HOLD) * RATE : n + 1 - DEPTH;
      static_for<c0, c1>([&](auto I) { consume(I); });
      if constexpr (c1 <= n - DEPTH) { touch(kv[n - DEPTH]); fence(); }
      issue(N);
    });
    STAMP(4);
    if (r < 2) *(LAS f32x4*)((LAS char*)lds + lw) = ao;
    wg_barrier();
    if (w < NPOLL) {
      const __amdgpu_buffer_rsrc_t rw = __builtin_amdgcn_make_buffer_rsrc((void*)p.ws, 0, WS_END, RSRC_W3);
      v4u used = *(const LAS v4u*)&lds[L_USED + (lane >> 3) * 4];
      wg_barrier();
      __builtin_amdgcn_s_sleep(PD0);
      for (int i = 0; i < w; ++i) __builtin_amdgcn_s_sleep(PSTAG);
      asm volatile("" : "+v"(used));
      v4u d0, d1, d2, d3;
      unsigned long long p0 = ~0ull, p1 = ~0ull, p2 = ~0ull, p3 = ~0ull, ta, tb;
      unsigned left = SPIN_CAP, fin, c0, c1;
      asm volatile(
          "1:\n\t"
          "s_mov_b64 exec, %[p0]\n\tbuffer_load_dwordx4 %[d0], %[od], %[rw], 0 offen sc1\n\t"
          "s_mov_b64 exec, %[p1]\n\tbuffer_load_dwordx4 %[d1], %[od], %[rw], 0 offen offset:1024 sc1\n\t"
          "s_mov_b64 exec, %[p2]\n\tbuffer_load_dwordx4 %[d2], %[od], %[rw], 0 offen offset:2048 sc1\n\t"
          "s_mov_b64 exec, %[p3]\n\tbuffer_load_dwordx4 %[d3], %[od], %[rw], 0 offen offset:3072 sc1\n\t"
          "s_mov_b64 exec, -1\n\t"
          "s_sub_u32 %[left], %[left], 1\n\t"
          "s_waitcnt vmcnt(0)\n\t"
          "ds_read_b32 %[fin], %[fa]\n\t"
          WCMP(v101, v103, p0) WCMP(v105, v107, p1) WCMP(v109, v111, p2) WCMP(v113, v115, p3)
          "s_or_b64 %[ta], %[p0], %[p1]\n\ts_or_b64 %[tb], %[p2], %[p3]\n\ts_or_b64 %[ta], %[ta], %[tb]\n\t"
          "s_cbranch_scc0 2f\n\t"
          "s_waitcnt lgkmcnt(0)\n\t"
          "v_cmp_ne_u32 vcc, 0, %[fin]\n\t"
          "s_cmp_eq_u32 %[left], 0\n\t"
          "s_cbranch_scc1 2f\n\t"
          "s_cbranch_vccz 1b\n\t"
          "2:\n\t"
          "s_waitcnt lgkmcnt(0)"
          : [d0] "=&{v[100:103]}"(d0), [d1] "=&{v[104:107]}"(d1), [d2] "=&{v[108:111]}"(d2), [d3] "=&{v[112:115]}"(d3), [p0] "+s"(p0), [p1] "+s"(p1), [p2] "+s"(p2), [p3] "+s"(p3),
            [ta] "=&s"(ta), [tb] "=&s"(tb), [left] "+s"(left), [fin] "=&v"(fin), [c0] "=&s"(c0), [c1] "=&s"(c1)
          : [od] "v"(od), [rw] "s"(rw), [nn] "v"(nonce), [fa] "v"(fa), [many] "i"(MANY), [pb] "i"(PB)
          : "vcc", "scc", "memory");
      if (ta == 0ull) {
        float s0 = (__uint_as_float(d0[0]) + __uint_as_float(d1[0])) + (__uint_as_float(d2[0]) + __uint_as_float(d3[0]));
        float s1 = (__uint_as_float(d0[2]) + __uint_as_float(d1[2])) + (__uint_as_float(d2[2]) + __uint_as_float(d3[2]));
        s0 += dppf<XOR8>(s0); s1 += dppf<XOR8>(s1);
        s0 = xrow_sum(s0); s1 = xrow_sum(s1);
        if (lane < 8) __builtin_amdgcn_raw_buffer_store_b32(cvtpk(s0, s1), rout, oo, 0, AUXOUT);
        if (lane == 0) lds[L_CNT + 5] = __uint_as_float(1u);
        const v4u rep = {d0[1] & 0xffffu, d1[1] & 0xffffu, d2[1] & 0xffffu, d3[1] & 0xffffu};
        if (rep[0] != used[0] || rep[1] != used[1] || rep[2] != used[2] || rep[3] != used[3]) {
          *(LAS v4u*)&lds[L_NEW + (lane >> 3) * 4] = rep;
          lds[L_CNT + 4] = __uint_as_float(1u);
        }
        STAMPW(5);
      }
    } else {
      wg_barrier();
    }
    wg_barrier();
  } else {
    USE_ALL_ARGS();
    const int ww = w - 4, j = h >> 3;
    const int ch0 = (NCH * hp) >> 5, nch = ((NCH * (hp + 1)) >> 5) - ch0;
    const __amdgpu_buffer_rsrc_t rw = __builtin_amdgcn_make_buffer_rsrc((void*)p.ws, 0, WS_END, RSRC_W3);
    const __amdgpu_buffer_rsrc_t rq = __builtin_amdgcn_make_buffer_rsrc((void*)(p.wq), 0, DM * NH * DH * 2, RSRC_W3);
    const int vq = (128 * ww + rp) * 8192 + h * 256 + c * 16;
    const GAS unsigned short* xb = (const GAS unsigned short*)p.x + b * DM;
    __builtin_amdgcn_s_setprio(3);
    const unsigned long long t0 = __builtin_amdgcn_s_memrealtime();
    if (lane < 8 && ww == 0) lds[L_CNT + lane] = 0.f;
    const unsigned xq0 = xb[128 * ww + 4 * ((c + 4 * b) & 31) + r], xq1 = xb[128 * ww + 4 * ((16 + c + 4 * b) & 31) + r];
    v4u wq[32], w1[16], w3[16], wo[32], w2[12];
    auto ldq = [&](auto I) { wq[I.value] = __builtin_amdgcn_raw_buffer_load_b128(rq, vq, (((I.value >> 1) + 2 * b) & 15) * 65536 + (I.value & 1) * 16384, AUXW); fence(); };
    static_for<0, 4>(ldq);
    const int odn = ww == 3 ? WS_DATA + (b * 32 + hp) * 4096 + lane * 16 : OOB;
    const unsigned oldn = __builtin_amdgcn_raw_buffer_load_b32(rw, ww == 3 ? 0 : OOB, WS_R + (b * 32 + hp) * 128, AUX_SC1);
    v4u inb4[4];
#pragma unroll
    for (int q = 0; q < 4; ++q) inb4[q] = __builtin_amdgcn_raw_buffer_load_b128(rw, odn + q * 1024, 0, AUX_SC1);
    fence();
    wg_barrier();
    const __amdgpu_buffer_rsrc_t r1 = __builtin_amdgcn_make_buffer_rsrc((void*)(p.w1), 0, DM * FF * 2, RSRC_W3);
    const __amdgpu_buffer_rsrc_t r3 = __builtin_amdgcn_make_buffer_rsrc((void*)(p.w3), 0, DM * FF * 2, RSRC_W3);
    const __amdgpu_buffer_rsrc_t ro = __builtin_amdgcn_make_buffer_rsrc((void*)(p.wo + (size_t)h * DH * DM), 0, DH * DM * 2, RSRC_W3);
    const __amdgpu_buffer_rsrc_t r2 = __builtin_amdgcn_make_buffer_rsrc((void*)(p.w2 + (size_t)ch0 * 8 * DM), 0, nch * 8 * DM * 2, RSRC_W3);
    const int v13 = ((c & 7) < nch) ? (128 * ww + 2 * r + (c >> 3)) * (FF * 2) + (ch0 + (c & 7)) * 16 : OOB;
    const int vo = rp * 1024 + ww * 256 + c * 16;
    const int rot2 = b >= 6 ? b - 6 : b;
    auto ld13 = [&](auto I) {
      w1[I.value] = __builtin_amdgcn_raw_buffer_load_b128(r1, v13, ((I.value + 2 * b) & 15) * (8 * FF * 2), AUXW);
      w3[I.value] = __builtin_amdgcn_raw_buffer_load_b128(r3, v13, ((I.value + 2 * b) & 15) * (8 * FF * 2), AUXW);
      fence();
    };
    auto ldo = [&](auto I) { wo[I.value] = __builtin_amdgcn_raw_buffer_load_b128(ro, vo + (I.value & 1) * 2048, (((I.value >> 1) + 2 * b) & 15) * 8192, AUXW); fence(); };
    auto ld2 = [&](auto I) { const int s = (I.value >> 1) + rot2; w2[I.value] = __builtin_amdgcn_raw_buffer_load_b128(r2, vo + (I.value & 1) * 2048, (s >= 6 ? s - 6 : s) * 8192, AUXW); fence(); };
    v4u sel;
    {
      const unsigned hb = (c >> 3) ? 0x0302u : 0x0100u;
      const unsigned s = (c & 1) ? ((hb << 16) | 0x0c0cu) : (0x0c0c0000u | hb);
#pragma unroll
      for (int d = 0; d < 4; ++d) sel[d] = (((c & 7) >> 1) == d) ? s : 0x0c0c0c0cu;
    }
    const unsigned xq[2] = {xq0 | (xq0 << 16), xq1 | (xq1 << 16)};
    unsigned xp;
    f32x4 aq = {0.f, 0.f, 0.f, 0.f}, ag = aq, au = aq, af = aq;
    auto useq = [&](auto I) { aq = rowsmf<((2 * I.value) & 15)>(xq[I.value >> 3], rs, wq[2 * I.value], wq[2 * I.value + 1], aq); fence(); };
    auto use13 = [&](auto I) { const v4u a = pair_a<I.value>(xp, sel); ag = mfma(a, w1[I.value], ag); au = mfma(a, w3[I.value], au); fence(); };
    static_for<0, 2>(useq);
    STAMP(12);
    pace(t0, T1 + S1 * j);
    __builtin_amdgcn_s_setprio(3);
    fence();
    unsigned inb[8];
#pragma unroll
    for (int q = 0; q < 4; ++q) { touch(inb4[q]); inb[2 * q] = inb4[q][1]; inb[2 * q + 1] = inb4[q][3]; }
    fence();
    static_for<4, 32>(ldq);
    xp = *(const GAS unsigned*)(xb + 128 * ww + 8 * ((c + 2 * b) & 15) + 2 * r);
    const v4u xg = *(const GAS v4u*)(xb + 8 * lane);
    const v4u gw = *(const GAS v4u*)((const GAS unsigned short*)p.gw + 8 * lane);
    fence();
    static_for<0, 2>(ld13);
    static_for<0, 2>(ld2);
    static_for<0, 4>(ldo);
    static_for<2, 16>(useq);
    if (r < 2) *(LAS f32x4*)&lds[L_Q + ww * 128 + 8 * c + 4 * r] = aq;
    lds_arrive(lds + L_CNT + 3, lane);
    STAMP(6);
    if (ww == 3) {
      unsigned nonce = __builtin_amdgcn_readfirstlane(oldn);
      for (;;) {
        nonce = (nonce + 1u) & 0xffffu;
        bool hit = false;
#pragma unroll
        for (int q = 0; q < 4; ++q) hit |= (inb[2 * q] >> 16) == nonce || (inb[2 * q + 1] >> 16) == nonce;
        if (!__any(hit)) break;
      }
      if (lane == 0) {
        __builtin_amdgcn_raw_buffer_store_b32(nonce, rw, 0, WS_R + (b * 32 + hp) * 128, AUX_SC1);
        lds[L_CNT + 2] = __uint_as_float(nonce);
      }
    }
    static_for<0, 2>(use13);
    float g;
    {
      float d = dot2(xg[0], gw[0], 0.f);
      d = dot2(xg[1], gw[1], d); d = dot2(xg[2], gw[2], d); d = dot2(xg[3], gw[3], d);
      d = wave_sum(d);
      g = 1.f / (1.f + __expf(-d));
      asm volatile("" : "+v"(g));
    }
    pace(t0, T2 + S2 * j);
    fence();
    static_for<2, 16>(ld13);
    static_for<2, 16>(use13);
    STAMP(13);
    if ((r >> 1) == (c >> 3)) {
      *(LAS f32x4*)&lds[L_GU + ww * 256 + (r >> 1) * 128 + 8 * (c & 7) + 4 * (r & 1)] = ag;
      *(LAS f32x4*)&lds[L_GU + ww * 256 + (r >> 1) * 128 + 64 + 8 * (c & 7) + 4 * (r & 1)] = au;
    }
    asm volatile("s_waitcnt vmcnt(0)" ::: "memory");
    lds_arrive(lds + L_CNT + 0, lane);
    lds_wait4(lds + L_CNT + 0);
    STAMP(7);
    unsigned hw;
    {
      const int s2 = (c >> 1) + rot2 >= 6 ? (c >> 1) + rot2 - 6 : (c >> 1) + rot2;
      const int f = (8 * s2 + 4 * (c & 1) + r) & 63;
      float gt = 0.f, up = 0.f;
#pragma unroll
      for (int u = 0; u < 8; ++u) { gt += lds[L_GU + u * 128 + f]; up += lds[L_GU + u * 128 + 64 + f]; }
      const float hv = gt / (1.f + __expf(-gt)) * up;
      hw = (c < 12 && f < 8 * nch) ? cvtpk(hv, hv) : 0u;
    }
    pace(t0, T3 + S3 * j);
    fence();
    static_for<2, 12>(ld2);
    static_for<4, 32>(ldo);
    const int cg = 8 * ww + 2 * (c >> 2) + (r & 1);
    unsigned tag = __builtin_amdgcn_raw_buffer_load_b32(rw, WS_R + (b * 32 + cg) * 128, 0, AUX_SC1) & 0xffffu;
    fence();
    static_for<0, 6>([&](auto I) { af = rowsmf<2 * I.value>(hw, rs, w2[2 * I.value], w2[2 * I.value + 1], af); fence(); });
    af *= (1.f - g);
    lds_wait4(lds + L_CNT + 1);
    float fw[4];
    {
      f32x2 st[4];
#pragma unroll
      for (int u = 0; u < 4; ++u) st[u] = *(const LAS f32x2*)&lds[L_ST + 2 * u];
      const float M = fmaxf(fmaxf(st[0][0], st[1][0]), fmaxf(st[2][0], st[3][0]));
      float L = 0.f;
#pragma unroll
      for (int u = 0; u < 4; ++u) { fw[u] = __builtin_amdgcn_exp2f(st[u][0] - M); L += fw[u] * st[u][1]; }
      const float inv = g / L;
#pragma unroll
      for (int u = 0; u < 4; ++u) { fw[u] *= inv; asm volatile("" : "+v"(fw[u])); }
    }
    if (r < 2 && (c & 3) == 0) { lds[L_USED + cg] = __uint_as_float(tag); lds[L_NEW + cg] = __uint_as_float(tag); }
    const unsigned mine = __float_as_uint(lds[L_CNT + 2]);
    tag = (tag << 16) | mine;
    int o1_ = WS_DATA + (b * 32 + cg) * 4096 + h * 128 + (r >> 1) * 64 + (c & 3) * 16;
    int lo0_ = (L_O + 4 * ((c + 4 * b) & 31) + r) * 4, lo1_ = (L_O + 4 * ((16 + c + 4 * b) & 31) + r) * 4;
    static_for<0, 32>([&](auto I) { touch(wo[I.value]); });
    asm volatile("" : "+v"(af), "+v"(tag), "+v"(o1_), "+v"(lo0_), "+v"(lo1_));
    STAMP(8);
    wg_barrier();
    STAMP(9);
    float pa[4], pb[4];
#pragma unroll
    for (int u = 0; u < 4; ++u) { pa[u] = *(const LAS float*)((const LAS char*)lds + lo0_ + u * 512); pb[u] = *(const LAS float*)((const LAS char*)lds + lo1_ + u * 512); }
    fence();
    const float o0 = fmaf(pa[0], fw[0], pa[1] * fw[1]) + fmaf(pa[2], fw[2], pa[3] * fw[3]);
    const float o1v = fmaf(pb[0], fw[0], pb[1] * fw[1]) + fmaf(pb[2], fw[2], pb[3] * fw[3]);
    const unsigned ow[2] = {cvtpk(o0, o0), cvtpk(o1v, o1v)};
    static_for<0, 16>([&](auto I) { af = rowsmf<((2 * I.value) & 15)>(ow[I.value >> 3], rs, wo[2 * I.value], wo[2 * I.value + 1], af); });
    v4u m1 = {__float_as_uint(r < 2 ? af[0] : af[2]), tag, __float_as_uint(r < 2 ? af[1] : af[3]), tag};
    __builtin_amdgcn_raw_buffer_store_b128(m1, rw, o1_, 0, AUX_SC1);
    wg_barrier();
    STAMP(10);
    wg_barrier();
    if (__float_as_uint(lds[L_CNT + 4]) != 0u) {
      tag = (__float_as_uint(lds[L_NEW + cg]) << 16) | mine;
      m1[1] = tag; m1[3] = tag;
      __builtin_amdgcn_raw_buffer_store_b128(m1, rw, o1_, 0, AUX_SC1);
    }
  }
}

extern "C" void kernel_launch(void* const* d_in, const int* in_sizes, int n_in, void* d_out, int out_size, void* d_ws,
                              size_t ws_size, hipStream_t stream) {
  Params p;
  p.x = (const unsigned short*)d_in[0];
  p.k = (const unsigned short*)d_in[1];
  p.v = (const unsigned short*)d_in[2];
  p.wq = (const unsigned short*)d_in[3];
  p.wo = (const unsigned short*)d_in[4];
  p.w1 = (const unsigned short*)d_in[6];
  p.w3 = (const unsigned short*)d_in[7];
  p.w2 = (const unsigned short*)d_in[8];
  p.gw = (const unsigned short*)d_in[9];
  p.out = (unsigned short*)d_out;
  p.ws = (unsigned char*)d_ws;
  hipLaunchKernelGGL(fused_kernel, dim3(NB * NH), dim3(NTHR), 0, stream, p);
}
```

```cpp
#include <hip/hip_runtime.h>
#include <hip/hip_bf16.h>
#include <stdint.h>

typedef unsigned v4u __attribute__((ext_vector_type(4)));
typedef unsigned v8u __attribute__((ext_vector_type(8)));
typedef float f32x4 __attribute__((ext_vector_type(4)));
typedef float f32x2 __attribute__((ext_vector_type(2)));
typedef __bf16 bf16x8 __attribute__((ext_vector_type(8)));
typedef __bf16 bf16x16 __attribute__((ext_vector_type(16)));
typedef __bf16 bf16x2 __attribute__((ext_vector_type(2)));
#define GAS __attribute__((address_space(1)))
#define LAS __attribute__((address_space(3)))
#define DEVI __device__ __forceinline__

#define DEPTH 10
#define HOLD 50
#define RATE 4
#define AUXKV 2
#define AUXW 0
#define T1 230
#define S1 90
#define T2 650
#define S2 140
#define T3 1200
#define S3 130
#define DLOW 0
#define DELTA 20
#define CFROM 72
#define PUBBAR 1
#define PD0 18
#define PB 0
#define MANY 64
#define NPOLL 1
#define PSTAG 5
#define AUXOUT 0
#define PRIO0 1
#define CODEPF 0
#define NSTAMP 0
#define STAMP(i)
#define STAMPW(i)

constexpr int NB = 8, DM = 512, NH = 32, SK = 4096, DH = 128, WK = 1024, FF = 1376;
constexpr int NCH = FF / 8;
constexpr int NTHR = 512;
constexpr int RSRC_W3 = 0x00020000;
constexpr int AUX_SC1 = 16;
constexpr int OOB = 0x40000000;
constexpr int WS_R = 0, WS_DATA = 256 * 128, WS_END = WS_DATA + 256 * 4096;
constexpr int SPIN_CAP = 1 << 18;

template <int N> struct IC { static constexpr int value = N; };
template <int LO, int HI, class Fn> DEVI void static_for(Fn&& f) {
  if constexpr (LO < HI) { f(IC<LO>{}); static_for<LO + 1, HI>(f); }
}
DEVI float dot2(unsigned a, unsigned b, float c) {
  return __builtin_amdgcn_fdot2_f32_bf16(__builtin_bit_cast(bf16x2, a), __builtin_bit_cast(bf16x2, b), c, false);
}
DEVI unsigned cvtpk(float lo, float hi) {
  bf16x2 r = {(__bf16)lo, (__bf16)hi};
  return __builtin_bit_cast(unsigned, r);
}
template <int CTRL> DEVI float dppf(float x) {
  return __builtin_bit_cast(float, __builtin_amdgcn_mov_dpp(__builtin_bit_cast(int, x), CTRL, 0xf, 0xf, true));
}
template <int CTRL> DEVI unsigned dppu(unsigned x) { return (unsigned)__builtin_amdgcn_mov_dpp((int)x, CTRL, 0xf, 0xf, true); }
constexpr int XOR1 = 0xB1, XOR2 = 0x4E, XOR7 = 0x141, XOR8 = 0x128, BCAST = 0x150;
DEVI float xrow_max(float x) {
  auto s = __builtin_amdgcn_permlane16_swap(__float_as_uint(x), __float_as_uint(x), false, false);
  x = fmaxf(__uint_as_float(s[0]), __uint_as_float(s[1]));
  auto t = __builtin_amdgcn_permlane32_swap(__float_as_uint(x), __float_as_uint(x), false, false);
  return fmaxf(__uint_as_float(t[0]), __uint_as_float(t[1]));
}
DEVI float xrow_sum(float x) {
  auto s = __builtin_amdgcn_permlane16_swap(__float_as_uint(x), __float_as_uint(x), false, false);
  x = __uint_as_float(s[0]) + __uint_as_float(s[1]);
  auto t = __builtin_amdgcn_permlane32_swap(__float_as_uint(x), __float_as_uint(x), false, false);
  return __uint_as_float(t[0]) + __uint_as_float(t[1]);
}
DEVI float wave_max(float x) {
  x = fmaxf(x, dppf<XOR1>(x)); x = fmaxf(x, dppf<XOR2>(x)); x = fmaxf(x, dppf<XOR7>(x)); x = fmaxf(x, dppf<XOR8>(x));
  return xrow_max(x);
}
DEVI float wave_sum(float x) {
  x += dppf<XOR1>(x); x += dppf<XOR2>(x); x += dppf<XOR7>(x); x += dppf<XOR8>(x);
  return xrow_sum(x);
}
DEVI f32x4 mfma(v4u a, v4u b, f32x4 acc) {
  return __builtin_amdgcn_mfma_f32_16x16x32_bf16(__builtin_bit_cast(bf16x8, a), __builtin_bit_cast(bf16x8, b), acc, 0, 0, 0);
}
template <int T> DEVI v4u pair_a(unsigned word, const v4u& sel) {
  const unsigned wb = dppu<BCAST + T>(word);
  return v4u{__builtin_amdgcn_perm(wb, wb, sel[0]), __builtin_amdgcn_perm(wb, wb, sel[1]),
             __builtin_amdgcn_perm(wb, wb, sel[2]), __builtin_amdgcn_perm(wb, wb, sel[3])};
}
struct RowSel { unsigned m0, m1; int idx; };
template <int T> DEVI f32x4 rowsmf(unsigned word, const RowSel& q, v4u b0, v4u b1, f32x4 acc) {
  const v4u a = {dppu<BCAST + T>(word) & q.m0, dppu<BCAST + T>(word) & q.m1, dppu<BCAST + T + 1>(word) & q.m0, dppu<BCAST + T + 1>(word) & q.m1};
  const v8u b = __builtin_shufflevector(b0, b1, 0, 1, 2, 3, 4, 5, 6, 7);
  return __builtin_amdgcn_smfmac_f32_16x16x64_bf16(__builtin_bit_cast(bf16x8, a), __builtin_bit_cast(bf16x16, b), acc, q.idx, 0, 0);
}
DEVI void touch(const v4u& x) { asm volatile("" :: "v"(x)); }
DEVI void fence() { __builtin_amdgcn_sched_barrier(0); }
DEVI void wg_barrier() { asm volatile("s_waitcnt lgkmcnt(0)\n\ts_barrier" ::: "memory"); }
DEVI void pace(unsigned long long t0, int ticks) {
  for (int i = 0; i < 4096 && (int)(__builtin_amdgcn_s_memrealtime() - t0) < ticks; ++i) __builtin_amdgcn_s_sleep(2);
}
DEVI void lds_arrive(LAS float* p, int lane) {
  asm volatile("" ::: "memory");
  if (lane == 0) __hip_atomic_fetch_add((LAS unsigned*)p, 1u, __ATOMIC_RELAXED, __HIP_MEMORY_SCOPE_WORKGROUP);
}
DEVI void lds_wait4(LAS float* p) {
  while (__hip_atomic_load((LAS unsigned*)p, __ATOMIC_RELAXED, __HIP_MEMORY_SCOPE_WORKGROUP) < 4u) __builtin_amdgcn_s_sleep(1);
  asm volatile("" ::: "memory");
}

#define WCMP(a, b, p) "v_cmp_ne_u32_sdwa %[ta], " #a ", %[nn] src0_sel:WORD_1 src1_sel:DWORD\n\tv_cmp_ne_u32_sdwa %[tb], " #b ", %[nn] src0_sel:WORD_1 src1_sel:DWORD\n\ts_or_b64 %[" #p "], %[ta], %[tb]\n\t"
constexpr unsigned long long sched4(int a, int b, int c, int d) {
  return (unsigned long long)(a + DELTA) | (unsigned long long)(b + DELTA) << 16 | (unsigned long long)(c + DELTA) << 32 | (unsigned long long)(d + DELTA) << 48;
}
constexpr unsigned long long SCHED[13] = {
    sched4(564, 529, 375, 374), sched4(775, 640, 610, 450), sched4(862, 728, 743, 653), sched4(964, 929, 840, 824), sched4(1045, 1023, 932, 925),
    sched4(1096, 1087, 1121, 969), sched4(1374, 1225, 1250, 1219), sched4(1448, 1301, 1325, 1308), sched4(1526, 1496, 1410, 1403),
    sched4(1612, 1590, 1621, 1485), sched4(1695, 1690, 1705, 1563), sched4(1782, 1780, 1786, 1780), sched4(1880, 1880, 1880, 1880)};
struct Params {
  const unsigned short *x, *k, *v, *wq, *wo, *w1, *w3, *w2, *gw;
  unsigned short* out;
  unsigned char* ws;
};

constexpr int L_Q = 0;
constexpr int L_GU = L_Q + 4 * 128;
constexpr int L_O = L_GU + 4 * 256;
constexpr int L_ST = L_O + 4 * 128;
constexpr int L_USED = L_ST + 8;
constexpr int L_NEW = L_USED + 32;
constexpr int L_CNT = L_NEW + 32;
constexpr int L_STAMP = L_CNT + 8;
constexpr int L_END = L_STAMP + 2 * 32;

__global__ void __launch_bounds__(NTHR, 2) __attribute__((amdgpu_waves_per_eu(2, 2))) fused_kernel(Params p) {
  __shared__ __attribute__((aligned(16))) float lds_[L_END];
  LAS float* lds = (LAS float*)lds_;
  const int tid = threadIdx.x, lane = tid & 63, w = __builtin_amdgcn_readfirstlane(tid >> 6);
  const int r = lane >> 4, c = lane & 15;
  const int bid = blockIdx.x, h = bid & 31, b = bid >> 5;
  const int hp = (h & 7) * 4 + (h >> 3);
  RowSel rs;
  {
    const unsigned hm = (c & 3) == 3 ? 0xffff0000u : 0x0000ffffu;
    rs.m0 = (c & 4) ? 0u : hm; rs.m1 = (c & 4) ? hm : 0u;
    rs.idx = (((c & 3) == 3 ? 0 : (c & 3)) | 12) * 0x1111;
  }
  const int rp = (r >> 1) + 4 * (r & 1);
#define USE_ALL_ARGS() asm volatile("" :: "s"(p.x), "s"(p.k), "s"(p.v), "s"(p.wq), "s"(p.wo), "s"(p.w1), "s"(p.w3), "s"(p.w2), "s"(p.gw), "s"(p.out), "s"(p.ws))

  if (__builtin_expect(w < 4, 1)) {
    USE_ALL_ARGS();
    STAMP(0);
    const size_t kvo = ((size_t)bid * SK + (SK - WK)) * DH;
    const __amdgpu_buffer_rsrc_t rk = __builtin_amdgcn_make_buffer_rsrc((void*)(p.k + kvo), 0, WK * DH * 2, RSRC_W3);
    const __amdgpu_buffer_rsrc_t rv = __builtin_amdgcn_make_buffer_rsrc((void*)(p.v + kvo), 0, WK * DH * 2, RSRC_W3);
    const int vk = w * 4096 + lane * 16, vv = w * 4096 + rp * 256 + c * 16;
    v4u kv[128];
    auto issue = [&](auto N) {
      constexpr int n = N.value;
      if constexpr (n < 64) kv[n] = __builtin_amdgcn_raw_buffer_load_b128(rk, vk + (n & 3) * 1024, (n >> 2) * 16384, AUXKV);
      else if constexpr (n < 128) kv[n] = __builtin_amdgcn_raw_buffer_load_b128(rv, vv + (n & 1) * 512 + (n & 2) * 1024, ((n >> 2) & 15) * 16384, AUXKV);
      fence();
    };
    static_for<0, DEPTH>([&](auto N) { issue(N); });
    wg_barrier();
    static_for<DEPTH, HOLD>([&](auto N) { touch(kv[N.value - DEPTH]); fence(); issue(N); });
    STAMP(1);
    lds_wait4(lds + L_CNT + 3);
    STAMP(2);
    v4u qv;
    {
      f32x4 q0 = *(const LAS f32x4*)&lds[L_Q + 8 * c], q1 = *(const LAS f32x4*)&lds[L_Q + 8 * c + 4];
#pragma unroll
      for (int u = 1; u < 4; ++u) { q0 += *(const LAS f32x4*)&lds[L_Q + u * 128 + 8 * c]; q1 += *(const LAS f32x4*)&lds[L_Q + u * 128 + 8 * c + 4]; }
      qv = v4u{cvtpk(q0[0], q0[1]), cvtpk(q0[2], q0[3]), cvtpk(q1[0], q1[1]), cvtpk(q1[2], q1[3])};
    }
    int od = WS_DATA + (b * 32 + hp) * 4096 + lane * 16, lw = (L_O + w * 128 + 8 * c + 4 * (r & 1)) * 4, fa = (int)(size_t)(lds + L_CNT + 5);
    int oo = (b * DM + 32 * (hp >> 1) + 4 * (hp & 1) + 8 * (lane & 3) + 2 * ((lane >> 2) & 1)) * 2;
    asm volatile("" : "+v"(od), "+v"(lw), "+v"(fa), "+v"(oo));
    const __amdgpu_buffer_rsrc_t rout = __builtin_amdgcn_make_buffer_rsrc((void*)p.out, 0, NB * DM * 2, RSRC_W3);
    unsigned nonce;
    float xs[16], sc[4];
    unsigned pw[4];
    f32x4 ao = {0.f, 0.f, 0.f, 0.f};
    auto consume = [&](auto I) {
      constexpr int i = I.value;
      if constexpr (i < 64) {
        float d = dot2(kv[i][0], qv[0], 0.f);
        d = dot2(kv[i][1], qv[1], d); d = dot2(kv[i][2], qv[2], d); d = dot2(kv[i][3], qv[3], d);
        xs[i & 15] = d;
        if constexpr ((i & 15) == 15) {
#define TR_STEP(HALF, CTRL) _Pragma("unroll") for (int u = 0; u < (HALF); ++u) { \
          const float lo_ = xs[u], hi_ = xs[u + (HALF)]; const float t1_ = lo_ + dppf<CTRL>(lo_), t2_ = hi_ + dppf<CTRL>(hi_); \
          xs[u] = (c & (HALF)) ? t2_ : t1_; }
          TR_STEP(8, XOR8) TR_STEP(4, XOR7) TR_STEP(2, XOR2) TR_STEP(1, XOR1)
#undef TR_STEP
          sc[i >> 4] = xs[0] * (0.08838834764831845f * 1.4426950408889634f);
        }
        if constexpr (i == 63) {
          const float mw = wave_max(fmaxf(fmaxf(sc[0], sc[1]), fmaxf(sc[2], sc[3])));
          float e[4];
#pragma unroll
          for (int u = 0; u < 4; ++u) { e[u] = __builtin_amdgcn_exp2f(sc[u] - mw); pw[u] = cvtpk(e[u], e[u]); }
          const float lw = wave_sum((e[0] + e[1]) + (e[2] + e[3]));
          if (lane == 0) *(LAS f32x2*)&lds[L_ST + 2 * w] = f32x2{mw, lw};
          lds_arrive(lds + L_CNT + 1, lane);
          STAMP(3);
        }
      } else {
        if constexpr (i == 100) { lds_wait4(lds + L_CNT + 0); nonce = __float_as_uint(lds[L_CNT + 2]); asm volatile("" : "+v"(nonce)); }
        if constexpr (i & 1) ao = rowsmf<((i - 1) & 15)>(pw[(i - 64) >> 4], rs, kv[i - 1], kv[i], ao);
        else touch(kv[i]);
      }
      fence();
    };
    static_for<HOLD, 128 + DEPTH>([&](auto N) {
      constexpr int n = N.value;
      constexpr int c0 = (n - HOLD) * RATE < n - DEPTH ? (n - HOLD) * RATE : n - DEPTH;
      constexpr int c1 = (n + 1 - HOLD) * RATE < n + 1 - DEPTH ? (n + 1 - HOLD) * RATE : n + 1 - DEPTH;
      static_for<c0, c1>([&](auto I) { consume(I); });
      if constexpr (c1 <= n - DEPTH) { touch(kv[n - DEPTH]); fence(); }
      issue(N);
    });
    STAMP(4);
    if (r < 2) *(LAS f32x4*)((LAS char*)lds + lw) = ao;
    wg_barrier();
    if (w < NPOLL) {
      const __amdgpu_buffer_rsrc_t rw = __builtin_amdgcn_make_buffer_rsrc((void*)p.ws, 0, WS_END, RSRC_W3);
      v4u used = *(const LAS v4u*)&lds[L_USED + (lane >> 3) * 4];
      wg_barrier();
      __builtin_amdgcn_s_sleep(PD0);
      for (int i = 0; i < w; ++i) __builtin_amdgcn_s_sleep(PSTAG);
      asm volatile("" : "+v"(used));
      v4u d0, d1, d2, d3;
      unsigned long long p0 = ~0ull, p1 = ~0ull, p2 = ~0ull, p3 = ~0ull, ta, tb;
      unsigned left = SPIN_CAP, fin, c0, c1;
      asm volatile(
          "1:\n\t"
          "s_mov_b64 exec, %[p0]\n\tbuffer_load_dwordx4 %[d0], %[od], %[rw], 0 offen sc1\n\t"
          "s_mov_b64 exec, %[p1]\n\tbuffer_load_dwordx4 %[d1], %[od], %[rw], 0 offen offset:1024 sc1\n\t"
          "s_mov_b64 exec, %[p2]\n\tbuffer_load_dwordx4 %[d2], %[od], %[rw], 0 offen offset:2048 sc1\n\t"
          "s_mov_b64 exec, %[p3]\n\tbuffer_load_dwordx4 %[d3], %[od], %[rw], 0 offen offset:3072 sc1\n\t"
          "s_mov_b64 exec, -1\n\t"
          "s_sub_u32 %[left], %[left], 1\n\t"
          "s_waitcnt vmcnt(0)\n\t"
          "ds_read_b32 %[fin], %[fa]\n\t"
          WCMP(v101, v103, p0) WCMP(v105, v107, p1) WCMP(v109, v111, p2) WCMP(v113, v115, p3)
          "s_or_b64 %[ta], %[p0], %[p1]\n\ts_or_b64 %[tb], %[p2], %[p3]\n\ts_or_b64 %[ta], %[ta], %[tb]\n\t"
          "s_cbranch_scc0 2f\n\t"
          "s_waitcnt lgkmcnt(0)\n\t"
          "v_cmp_ne_u32 vcc, 0, %[fin]\n\t"
          "s_cmp_eq_u32 %[left], 0\n\t"
          "s_cbranch_scc1 2f\n\t"
          "s_cbranch_vccz 1b\n\t"
          "2:\n\t"
          "s_waitcnt lgkmcnt(0)"
          : [d0] "=&{v[100:103]}"(d0), [d1] "=&{v[104:107]}"(d1), [d2] "=&{v[108:111]}"(d2), [d3] "=&{v[112:115]}"(d3), [p0] "+s"(p0), [p1] "+s"(p1), [p2] "+s"(p2), [p3] "+s"(p3),
            [ta] "=&s"(ta), [tb] "=&s"(tb), [left] "+s"(left), [fin] "=&v"(fin), [c0] "=&s"(c0), [c1] "=&s"(c1)
          : [od] "v"(od), [rw] "s"(rw), [nn] "v"(nonce), [fa] "v"(fa), [many] "i"(MANY), [pb] "i"(PB)
          : "vcc", "scc", "memory");
      if (ta == 0ull) {
        float s0 = (__uint_as_float(d0[0]) + __uint_as_float(d1[0])) + (__uint_as_float(d2[0]) + __uint_as_float(d3[0]));
        float s1 = (__uint_as_float(d0[2]) + __uint_as_float(d1[2])) + (__uint_as_float(d2[2]) + __uint_as_float(d3[2]));
        s0 += dppf<XOR8>(s0); s1 += dppf<XOR8>(s1);
        s0 = xrow_sum(s0); s1 = xrow_sum(s1);
        if (lane < 8) __builtin_amdgcn_raw_buffer_store_b32(cvtpk(s0, s1), rout, oo, 0, AUXOUT);
        if (lane == 0) lds[L_CNT + 5] = __uint_as_float(1u);
        const v4u rep = {d0[1] & 0xffffu, d1[1] & 0xffffu, d2[1] & 0xffffu, d3[1] & 0xffffu};
        if (rep[0] != used[0] || rep[1] != used[1] || rep[2] != used[2] || rep[3] != used[3]) {
          *(LAS v4u*)&lds[L_NEW + (lane >> 3) * 4] = rep;
          lds[L_CNT + 4] = __uint_as_float(1u);
        }
        STAMPW(5);
      }
    } else {
      wg_barrier();
    }
    wg_barrier();
  } else {
    USE_ALL_ARGS();
    const int ww = w - 4, j = h >> 3;
    const int ch0 = (NCH * hp) >> 5, nch = ((NCH * (hp + 1)) >> 5) - ch0;
    const __amdgpu_buffer_rsrc_t rw = __builtin_amdgcn_make_buffer_rsrc((void*)p.ws, 0, WS_END, RSRC_W3);
    const __amdgpu_buffer_rsrc_t rq = __builtin_amdgcn_make_buffer_rsrc((void*)(p.wq), 0, DM * NH * DH * 2, RSRC_W3);
    const int vq = (128 * ww + rp) * 8192 + h * 256 + c * 16;
    const GAS unsigned short* xb = (const GAS unsigned short*)p.x + b * DM;
    __builtin_amdgcn_s_setprio(3);
    const unsigned long long t0 = __builtin_amdgcn_s_memrealtime();
    if (lane < 8 && ww == 0) lds[L_CNT + lane] = 0.f;
    const unsigned xq0 = xb[128 * ww + 4 * ((c + 4 * b) & 31) + r], xq1 = xb[128 * ww + 4 * ((16 + c + 4 * b) & 31) + r];
    v4u wq[32], w1[16], w3[16], wo[32], w2[12];
    auto ldq = [&](auto I) { wq[I.value] = __builtin_amdgcn_raw_buffer_load_b128(rq, vq, (((I.value >> 1) + 2 * b) & 15) * 65536 + (I.value & 1) * 16384, AUXW); fence(); };
    static_for<0, 4>(ldq);
    const int odn = ww == 3 ? WS_DATA + (b * 32 + hp) * 4096 + lane * 16 : OOB;
    const unsigned oldn = __builtin_amdgcn_raw_buffer_load_b32(rw, ww == 3 ? 0 : OOB, WS_R + (b * 32 + hp) * 128, AUX_SC1);
    v4u inb4[4];
#pragma unroll
    for (int q = 0; q < 4; ++q) inb4[q] = __builtin_amdgcn_raw_buffer_load_b128(rw, odn + q * 1024, 0, AUX_SC1);
    fence();
    wg_barrier();
    const __amdgpu_buffer_rsrc_t r1 = __builtin_amdgcn_make_buffer_rsrc((void*)(p.w1), 0, DM * FF * 2, RSRC_W3);
    const __amdgpu_buffer_rsrc_t r3 = __builtin_amdgcn_make_buffer_rsrc((void*)(p.w3), 0, DM * FF * 2, RSRC_W3);
    const __amdgpu_buffer_rsrc_t ro = __builtin_amdgcn_make_buffer_rsrc((void*)(p.wo + (size_t)h * DH * DM), 0, DH * DM * 2, RSRC_W3);
    const __amdgpu_buffer_rsrc_t r2 = __builtin_amdgcn_make_buffer_rsrc((void*)(p.w2 + (size_t)ch0 * 8 * DM), 0, nch * 8 * DM * 2, RSRC_W3);
    const int v13 = ((c & 7) < nch) ? (128 * ww + 2 * r + (c >> 3)) * (FF * 2) + (ch0 + (c & 7)) * 16 : OOB;
    const int vo = rp * 1024 + ww * 256 + c * 16;
    const int rot2 = b >= 6 ? b - 6 : b;
    auto ld13 = [&](auto I) {
      w1[I.value] = __builtin_amdgcn_raw_buffer_load_b128(r1, v13, ((I.value + 2 * b) & 15) * (8 * FF * 2), AUXW);
      w3[I.value] = __builtin_amdgcn_raw_buffer_load_b128(r3, v13, ((I.value + 2 * b) & 15) * (8 * FF * 2), AUXW);
      fence();
    };
    auto ldo = [&](auto I) { wo[I.value] = __builtin_amdgcn_raw_buffer_load_b128(ro, vo + (I.value & 1) * 2048, (((I.value >> 1) + 2 * b) & 15) * 8192, AUXW); fence(); };
    auto ld2 = [&](auto I) { const int s = (I.value >> 1) + rot2; w2[I.value] = __builtin_amdgcn_raw_buffer_load_b128(r2, vo + (I.value & 1) * 2048, (s >= 6 ? s - 6 : s) * 8192, AUXW); fence(); };
    v4u sel;
    {
      const unsigned hb = (c >> 3) ? 0x0302u : 0x0100u;
      const unsigned s = (c & 1) ? ((hb << 16) | 0x0c0cu) : (0x0c0c0000u | hb);
#pragma unroll
      for (int d = 0; d < 4; ++d) sel[d] = (((c & 7) >> 1) == d) ? s : 0x0c0c0c0cu;
    }
    const unsigned xq[2] = {xq0 | (xq0 << 16), xq1 | (xq1 << 16)};
    unsigned xp;
    f32x4 aq = {0.f, 0.f, 0.f, 0.f}, ag = aq, au = aq, af = aq;
    auto useq = [&](auto I) { aq = rowsmf<((2 * I.value) & 15)>(xq[I.value >> 3], rs, wq[2 * I.value], wq[2 * I.value + 1], aq); fence(); };
    auto use13 = [&](auto I) { const v4u a = pair_a<I.value>(xp, sel); ag = mfma(a, w1[I.value], ag); au = mfma(a, w3[I.value], au); fence(); };
    static_for<0, 2>(useq);
    STAMP(12);
    pace(t0, T1 + S1 * j);
    __builtin_amdgcn_s_setprio(3);
    fence();
    unsigned inb[8];
#pragma unroll
    for (int q = 0; q < 4; ++q) { touch(inb4[q]); inb[2 * q] = inb4[q][1]; inb[2 * q + 1] = inb4[q][3]; }
    fence();
    static_for<4, 32>(ldq);
    xp = *(const GAS unsigned*)(xb + 128 * ww + 8 * ((c + 2 * b) & 15) + 2 * r);
    const v4u xg = *(const GAS v4u*)(xb + 8 * lane);
    const v4u gw = *(const GAS v4u*)((const GAS unsigned short*)p.gw + 8 * lane);
    fence();
    static_for<0, 2>(ld13);
    static_for<0, 2>(ld2);
    static_for<0, 4>(ldo);
    static_for<2, 16>(useq);
    if (r < 2) *(LAS f32x4*)&lds[L_Q + ww * 128 + 8 * c + 4 * r] = aq;
    lds_arrive(lds + L_CNT + 3, lane);
    STAMP(6);
    if (ww == 3) {
      unsigned nonce = __builtin_amdgcn_readfirstlane(oldn);
      for (;;) {
        nonce = (nonce + 1u) & 0xffffu;
        bool hit = false;
#pragma unroll
        for (int q = 0; q < 4; ++q) hit |= (inb[2 * q] >> 16) == nonce || (inb[2 * q + 1] >> 16) == nonce;
        if (!__any(hit)) break;
      }
      if (lane == 0) {
        __builtin_amdgcn_raw_buffer_store_b32(nonce, rw, 0, WS_R + (b * 32 + hp) * 128, AUX_SC1);
        lds[L_CNT + 2] = __uint_as_float(nonce);
      }
    }
    static_for<0, 2>(use13);
    float g;
    {
      float d = dot2(xg[0], gw[0], 0.f);
      d = dot2(xg[1], gw[1], d); d = dot2(xg[2], gw[2], d); d = dot2(xg[3], gw[3], d);
      d = wave_sum(d);
      g = 1.f / (1.f + __expf(-d));
      asm volatile("" : "+v"(g));
    }
    pace(t0, T2 + S2 * j);
    fence();
    static_for<2, 16>(ld13);
    static_for<2, 16>(use13);
    STAMP(13);
    if ((r >> 1) == (c >> 3)) {
      *(LAS f32x4*)&lds[L_GU + ww * 256 + (r >> 1) * 128 + 8 * (c & 7) + 4 * (r & 1)] = ag;
      *(LAS f32x4*)&lds[L_GU + ww * 256 + (r >> 1) * 128 + 64 + 8 * (c & 7) + 4 * (r & 1)] = au;
    }
    asm volatile("s_waitcnt vmcnt(0)" ::: "memory");
    lds_arrive(lds + L_CNT + 0, lane);
    lds_wait4(lds + L_CNT + 0);
    STAMP(7);
    unsigned hw;
    {
      const int s2 = (c >> 1) + rot2 >= 6 ? (c >> 1) + rot2 - 6 : (c >> 1) + rot2;
      const int f = (8 * s2 + 4 * (c & 1) + r) & 63;
      float gt = 0.f, up = 0.f;
#pragma unroll
      for (int u = 0; u < 8; ++u) { gt += lds[L_GU + u * 128 + f]; up += lds[L_GU + u * 128 + 64 + f]; }
      const float hv = gt / (1.f + __expf(-gt)) * up;
      hw = (c < 12 && f < 8 * nch) ? cvtpk(hv, hv) : 0u;
    }
    pace(t0, T3 + S3 * j);
    fence();
    static_for<2, 12>(ld2);
    static_for<4, 32>(ldo);
    const int cg = 8 * ww + 2 * (c >> 2) + (r & 1);
    unsigned tag = __builtin_amdgcn_raw_buffer_load_b32(rw, WS_R + (b * 32 + cg) * 128, 0, AUX_SC1) & 0xffffu;
    fence();
    static_for<0, 6>([&](auto I) { af = rowsmf<2 * I.value>(hw, rs, w2[2 * I.value], w2[2 * I.value + 1], af); fence(); });
    af *= (1.f - g);
    lds_wait4(lds + L_CNT + 1);
    float fw[4];
    {
      f32x2 st[4];
#pragma unroll
      for (int u = 0; u < 4; ++u) st[u] = *(const LAS f32x2*)&lds[L_ST + 2 * u];
      const float M = fmaxf(fmaxf(st[0][0], st[1][0]), fmaxf(st[2][0], st[3][0]));
      float L = 0.f;
#pragma unroll
      for (int u = 0; u < 4; ++u) { fw[u] = __builtin_amdgcn_exp2f(st[u][0] - M); L += fw[u] * st[u][1]; }
      const float inv = g / L;
#pragma unroll
      for (int u = 0; u < 4; ++u) { fw[u] *= inv; asm volatile("" : "+v"(fw[u])); }
    }
    if (r < 2 && (c & 3) == 0) { lds[L_USED + cg] = __uint_as_float(tag); lds[L_NEW + cg] = __uint_as_float(tag); }
    const unsigned mine = __float_as_uint(lds[L_CNT + 2]);
    tag = (tag << 16) | mine;
    int o1_ = WS_DATA + (b * 32 + cg) * 4096 + h * 128 + (r >> 1) * 64 + (c & 3) * 16;
    int lo0_ = (L_O + 4 * ((c + 4 * b) & 31) + r) * 4, lo1_ = (L_O + 4 * ((16 + c + 4 * b) & 31) + r) * 4;
    static_for<0, 32>([&](auto I) { touch(wo[I.value]); });
    asm volatile("" : "+v"(af), "+v"(tag), "+v"(o1_), "+v"(lo0_), "+v"(lo1_));
    STAMP(8);
    wg_barrier();
    STAMP(9);
    float pa[4], pb[4];
#pragma unroll
    for (int u = 0; u < 4; ++u) { pa[u] = *(const LAS float*)((const LAS char*)lds + lo0_ + u * 512); pb[u] = *(const LAS float*)((const LAS char*)lds + lo1_ + u * 512); }
    fence();
    const float o0 = fmaf(pa[0], fw[0], pa[1] * fw[1]) + fmaf(pa[2], fw[2], pa[3] * fw[3]);
    const float o1v = fmaf(pb[0], fw[0], pb[1] * fw[1]) + fmaf(pb[2], fw[2], pb[3] * fw[3]);
    const unsigned ow[2] = {cvtpk(o0, o0), cvtpk(o1v, o1v)};
    static_for<0, 16>([&](auto I) { af = rowsmf<((2 * I.value) & 15)>(ow[I.value >> 3], rs, wo[2 * I.value], wo[2 * I.value + 1], af); });
    v4u m1 = {__float_as_uint(r < 2 ? af[0] : af[2]), tag, __float_as_uint(r < 2 ? af[1] : af[3]), tag};
    __builtin_amdgcn_raw_buffer_store_b128(m1, rw, o1_, 0, AUX_SC1);
    wg_barrier();
    STAMP(10);
    wg_barrier();
    if (__float_as_uint(lds[L_CNT + 4]) != 0u) {
      tag = (__float_as_uint(lds[L_NEW + cg]) << 16) | mine;
      m1[1] = tag; m1[3] = tag;
      __builtin_amdgcn_raw_buffer_store_b128(m1, rw, o1_, 0, AUX_SC1);
    }
  }
}

extern "C" void kernel_launch(void* const* d_in, const int* in_sizes, int n_in, void* d_out, int out_size, void* d_ws,
                              size_t ws_size, hipStream_t stream) {
  Params p;
  p.x = (const unsigned short*)d_in[0];
  p.k = (const unsigned short*)d_in[1];
  p.v = (const unsigned short*)d_in[2];
  p.wq = (const unsigned short*)d_in[3];
  p.wo = (const unsigned short*)d_in[4];
  p.w1 = (const unsigned short*)d_in[6];
  p.w3 = (const unsigned short*)d_in[7];
  p.w2 = (const unsigned short*)d_in[8];
  p.gw = (const unsigned short*)d_in[9];
  p.out = (unsigned short*)d_out;
  p.ws = (unsigned char*)d_ws;
  hipLaunchKernelGGL(fused_kernel, dim3(NB * NH), dim3(NTHR), 0, stream, p);
}
```

```cpp
#include <hip/hip_runtime.h>
#include <hip/hip_bf16.h>
#include <stdint.h>

typedef unsigned v4u __attribute__((ext_vector_type(4)));
typedef unsigned v8u __attribute__((ext_vector_type(8)));
typedef float f32x4 __attribute__((ext_vector_type(4)));
typedef float f32x2 __attribute__((ext_vector_type(2)));
typedef __bf16 bf16x8 __attribute__((ext_vector_type(8)));
typedef __bf16 bf16x16 __attribute__((ext_vector_type(16)));
typedef __bf16 bf16x2 __attribute__((ext_vector_type(2)));
#define GAS __attribute__((address_space(1)))
#define LAS __attribute__((address_space(3)))
#define DEVI __device__ __forceinline__

#define DEPTH 10
#define HOLD 50
#define RATE 4
#define AUXKV 2
#define AUXW 0
#define T1 230
#define S1 90
#define T2 650
#define S2 140
#define T3 1200
#define S3 130
#define DLOW 5
#define DELTA 20
#define CFROM 72
#define PUBBAR 1
#define PD0 18
#define PB 0
#define MANY 64
#define NPOLL 1
#define PSTAG 5
#define AUXOUT 0
#define PRIO0 1
#define CODEPF 0
#define NSTAMP 30
#define STAMP(i) do { __builtin_amdgcn_sched_barrier(0); const unsigned long long t_ = __builtin_amdgcn_s_memrealtime(); __builtin_amdgcn_s_waitcnt(0xC07F); \
  if (lane == 0 && (w & 3) == 0) ((LAS unsigned long long*)(lds + L_STAMP))[i] = t_; __builtin_amdgcn_sched_barrier(0); } while (0)
#define STAMPW(i) do { const unsigned long long t_ = __builtin_amdgcn_s_memrealtime(); __builtin_amdgcn_s_waitcnt(0xC07F); \
  if (lane == 0 && ((LAS unsigned long long*)(lds + L_STAMP))[i] == 0ull) ((LAS unsigned long long*)(lds + L_STAMP))[i] = t_; } while (0)

constexpr int NB = 8, DM = 512, NH = 32, SK = 4096, DH = 128, WK = 1024, FF = 1376;
constexpr int NCH = FF / 8;
constexpr int NTHR = 512;
constexpr int RSRC_W3 = 0x00020000;
constexpr int AUX_SC1 = 16;
constexpr int OOB = 0x40000000;
constexpr int WS_R = 0, WS_DATA = 256 * 128, WS_END = WS_DATA + 256 * 4096;
constexpr int SPIN_CAP = 1 << 18;

template <int N> struct IC { static constexpr int value = N; };
template <int LO, int HI, class Fn> DEVI void static_for(Fn&& f) {
  if constexpr (LO < HI) { f(IC<LO>{}); static_for<LO + 1, HI>(f); }
}
DEVI float dot2(unsigned a, unsigned b, float c) {
  return __builtin_amdgcn_fdot2_f32_bf16(__builtin_bit_cast(bf16x2, a), __builtin_bit_cast(bf16x2, b), c, false);
}
DEVI unsigned cvtpk(float lo, float hi) {
  bf16x2 r = {(__bf16)lo, (__bf16)hi};
  return __builtin_bit_cast(unsigned, r);
}
template <int CTRL> DEVI float dppf(float x) {
  return __builtin_bit_cast(float, __builtin_amdgcn_mov_dpp(__builtin_bit_cast(int, x), CTRL, 0xf, 0xf, true));
}
template <int CTRL> DEVI unsigned dppu(unsigned x) { return (unsigned)__builtin_amdgcn_mov_dpp((int)x, CTRL, 0xf, 0xf, true); }
constexpr int XOR1 = 0xB1, XOR2 = 0x4E, XOR7 = 0x141, XOR8 = 0x128, BCAST = 0x150;
DEVI float xrow_max(float x) {
  auto s = __builtin_amdgcn_permlane16_swap(__float_as_uint(x), __float_as_uint(x), false, false);
  x = fmaxf(__uint_as_float(s[0]), __uint_as_float(s[1]));
  auto t = __builtin_amdgcn_permlane32_swap(__float_as_uint(x), __float_as_uint(x), false, false);
  return fmaxf(__uint_as_float(t[0]), __uint_as_float(t[1]));
}
DEVI float xrow_sum(float x) {
  auto s = __builtin_amdgcn_permlane16_swap(__float_as_uint(x), __float_as_uint(x), false, false);
  x = __uint_as_float(s[0]) + __uint_as_float(s[1]);
  auto t = __builtin_amdgcn_permlane32_swap(__float_as_uint(x), __float_as_uint(x), false, false);
  return __uint_as_float(t[0]) + __uint_as_float(t[1]);
}
DEVI float wave_max(float x) {
  x = fmaxf(x, dppf<XOR1>(x)); x = fmaxf(x, dppf<XOR2>(x)); x = fmaxf(x, dppf<XOR7>(x)); x = fmaxf(x, dppf<XOR8>(x));
  return xrow_max(x);
}
DEVI float wave_sum(float x) {
  x += dppf<XOR1>(x); x += dppf<XOR2>(x); x += dppf<XOR7>(x); x += dppf<XOR8>(x);
  return xrow_sum(x);
}
DEVI f32x4 mfma(v4u a, v4u b, f32x4 acc) {
  return __builtin_amdgcn_mfma_f32_16x16x32_bf16(__builtin_bit_cast(bf16x8, a), __builtin_bit_cast(bf16x8, b), acc, 0, 0, 0);
}
template <int T> DEVI v4u pair_a(unsigned word, const v4u& sel) {
  const unsigned wb = dppu<BCAST + T>(word);
  return v4u{__builtin_amdgcn_perm(wb, wb, sel[0]), __builtin_amdgcn_perm(wb, wb, sel[1]),
             __builtin_amdgcn_perm(wb, wb, sel[2]), __builtin_amdgcn_perm(wb, wb, sel[3])};
}
struct RowSel { unsigned m0, m1; int idx; };
template <int T> DEVI f32x4 rowsmf(unsigned word, const RowSel& q, v4u b0, v4u b1, f32x4 acc) {
  const v4u a = {dppu<BCAST + T>(word) & q.m0, dppu<BCAST + T>(word) & q.m1, dppu<BCAST + T + 1>(word) & q.m0, dppu<BCAST + T + 1>(word) & q.m1};
  const v8u b = __builtin_shufflevector(b0, b1, 0, 1, 2, 3, 4, 5, 6, 7);
  return __builtin_amdgcn_smfmac_f32_16x16x64_bf16(__builtin_bit_cast(bf16x8, a), __builtin_bit_cast(bf16x16, b), acc, q.idx, 0, 0);
}
DEVI void touch(const v4u& x) { asm volatile("" :: "v"(x)); }
DEVI void fence() { __builtin_amdgcn_sched_barrier(0); }
DEVI void wg_barrier() { asm volatile("s_waitcnt lgkmcnt(0)\n\ts_barrier" ::: "memory"); }
DEVI void pace(unsigned long long t0, int ticks) {
  for (int i = 0; i < 4096 && (int)(__builtin_amdgcn_s_memrealtime() - t0) < ticks; ++i) __builtin_amdgcn_s_sleep(2);
}
DEVI void lds_arrive(LAS float* p, int lane) {
  asm volatile("" ::: "memory");
  if (lane == 0) __hip_atomic_fetch_add((LAS unsigned*)p, 1u, __ATOMIC_RELAXED, __HIP_MEMORY_SCOPE_WORKGROUP);
}
DEVI void lds_wait4(LAS float* p) {
  while (__hip_atomic_load((LAS unsigned*)p, __ATOMIC_RELAXED, __HIP_MEMORY_SCOPE_WORKGROUP) < 4u) __builtin_amdgcn_s_sleep(1);
  asm volatile("" ::: "memory");
}

#define WCMP(a, b, p) "v_cmp_ne_u32_sdwa %[ta], " #a ", %[nn] src0_sel:WORD_1 src1_sel:DWORD\n\tv_cmp_ne_u32_sdwa %[tb], " #b ", %[nn] src0_sel:WORD_1 src1_sel:DWORD\n\ts_or_b64 %[" #p "], %[ta], %[tb]\n\t"
constexpr unsigned long long sched4(int a, int b, int c, int d) {
  return (unsigned long long)(a + DELTA) | (unsigned long long)(b + DELTA) << 16 | (unsigned long long)(c + DELTA) << 32 | (unsigned long long)(d + DELTA) << 48;
}
constexpr unsigned long long SCHED[13] = {
    sched4(564, 529, 375, 374), sched4(775, 640, 610, 450), sched4(862, 728, 743, 653), sched4(964, 929, 840, 824), sched4(1045, 1023, 932, 925),
    sched4(1096, 1087, 1121, 969), sched4(1374, 1225, 1250, 1219), sched4(1448, 1301, 1325, 1308), sched4(1526, 1496, 1410, 1403),
    sched4(1612, 1590, 1621, 1485), sched4(1695, 1690, 1705, 1563), sched4(1782, 1780, 1786, 1780), sched4(1880, 1880, 1880, 1880)};
struct Params {
  const unsigned short *x, *k, *v, *wq, *wo, *w1, *w3, *w2, *gw;
  unsigned short* out;
  unsigned char* ws;
};

constexpr int L_Q = 0;
constexpr int L_GU = L_Q + 4 * 128;
constexpr int L_O = L_GU + 4 * 256;
constexpr int L_ST = L_O + 4 * 128;
constexpr int L_USED = L_ST + 8;
constexpr int L_NEW = L_USED + 32;
constexpr int L_CNT = L_NEW + 32;
constexpr int L_STAMP = L_CNT + 8;
constexpr int L_END = L_STAMP + 2 * 32;

__global__ void __launch_bounds__(NTHR, 2) __attribute__((amdgpu_waves_per_eu(2, 2))) fused_kernel(Params p) {
  __shared__ __attribute__((aligned(16))) float lds_[L_END];
  LAS float* lds = (LAS float*)lds_;
  const int tid = threadIdx.x, lane = tid & 63, w = __builtin_amdgcn_readfirstlane(tid >> 6);
  const int r = lane >> 4, c = lane & 15;
  const int bid = blockIdx.x, h = bid & 31, b = bid >> 5;
  const int hp = (h & 7) * 4 + (h >> 3);
  RowSel rs;
  {
    const unsigned hm = (c & 3) == 3 ? 0xffff0000u : 0x0000ffffu;
    rs.m0 = (c & 4) ? 0u : hm; rs.m1 = (c & 4) ? hm : 0u;
    rs.idx = (((c & 3) == 3 ? 0 : (c & 3)) | 12) * 0x1111;
  }
  const int rp = (r >> 1) + 4 * (r & 1);
#define USE_ALL_ARGS() asm volatile("" :: "s"(p.x), "s"(p.k), "s"(p.v), "s"(p.wq), "s"(p.wo), "s"(p.w1), "s"(p.w3), "s"(p.w2), "s"(p.gw), "s"(p.out), "s"(p.ws))

  if (__builtin_expect(w < 4, 1)) {
    USE_ALL_ARGS();
    STAMP(0);
    if (tid == 0) ((LAS unsigned long long*)(lds + L_STAMP))[5] = 0ull;
    const size_t kvo = ((size_t)bid * SK + (SK - WK)) * DH;
    const __amdgpu_buffer_rsrc_t rk = __builtin_amdgcn_make_buffer_rsrc((void*)(p.k + kvo), 0, WK * DH * 2, RSRC_W3);
    const __amdgpu_buffer_rsrc_t rv = __builtin_amdgcn_make_buffer_rsrc((void*)(p.v + kvo), 0, WK * DH * 2, RSRC_W3);
    const int vk = w * 4096 + lane * 16, vv = w * 4096 + rp * 256 + c * 16;
    v4u kv[128];
    const unsigned ts0 = (unsigned)__builtin_amdgcn_s_memrealtime();
    unsigned tnow = 0, tel = 1, tth = 0;
    auto issue = [&](auto N) {
      constexpr int n = N.value;
      if constexpr (n >= CFROM && n < 128) {
        if constexpr ((n & 7) == 0) tnow = (unsigned)__builtin_amdgcn_s_memrealtime();
        if constexpr ((n & 7) == 1) { tel = tnow - ts0; tth = (unsigned)(SCHED[n / 8 - 3] >> (16 * (h >> 3))) & 0xffffu; }
        if constexpr (n > CFROM) asm volatile("s_cmp_lt_u32 %0, %1\n\ts_cbranch_scc0 1f\n\ts_waitcnt vmcnt(%2)\n1:" :: "s"(tel), "s"(tth), "i"(DLOW - 1) : "scc");
        fence();
      }
      if constexpr (n < 64) kv[n] = __builtin_amdgcn_raw_buffer_load_b128(rk, vk + (n & 3) * 1024, (n >> 2) * 16384, AUXKV);
      else if constexpr (n < 128) kv[n] = __builtin_amdgcn_raw_buffer_load_b128(rv, vv + (n & 1) * 512 + (n & 2) * 1024, ((n >> 2) & 15) * 16384, AUXKV);
      fence();
      if constexpr (n >= 16 && n < 128 && (n & 7) == 0) STAMP(14 + n / 8);
    };
    static_for<0, DEPTH>([&](auto N) { issue(N); });
    wg_barrier();
    touch(kv[0]); STAMP(11);
    static_for<DEPTH, HOLD>([&](auto N) { touch(kv[N.value - DEPTH]); fence(); issue(N); });
    STAMP(1);
    lds_wait4(lds + L_CNT + 3);
    STAMP(2);
    v4u qv;
    {
      f32x4 q0 = *(const LAS f32x4*)&lds[L_Q + 8 * c], q1 = *(const LAS f32x4*)&lds[L_Q + 8 * c + 4];
#pragma unroll
      for (int u = 1; u < 4; ++u) { q0 += *(const LAS f32x4*)&lds[L_Q + u * 128 + 8 * c]; q1 += *(const LAS f32x4*)&lds[L_Q + u * 128 + 8 * c + 4]; }
      qv = v4u{cvtpk(q0[0], q0[1]), cvtpk(q0[2], q0[3]), cvtpk(q1[0], q1[1]), cvtpk(q1[2], q1[3])};
    }
    int od = WS_DATA + (b * 32 + hp) * 4096 + lane * 16, lw = (L_O + w * 128 + 8 * c + 4 * (r & 1)) * 4, fa = (int)(size_t)(lds + L_CNT + 5);
    int oo = (b * DM + 32 * (hp >> 1) + 4 * (hp & 1) + 8 * (lane & 3) + 2 * ((lane >> 2) & 1)) * 2;
    asm volatile("" : "+v"(od), "+v"(lw), "+v"(fa), "+v"(oo));
    const __amdgpu_buffer_rsrc_t rout = __builtin_amdgcn_make_buffer_rsrc((void*)p.out, 0, NB * DM * 2, RSRC_W3);
    unsigned nonce;
    float xs[16], sc[4];
    unsigned pw[4];
    f32x4 ao = {0.f, 0.f, 0.f, 0.f};
    auto consume = [&](auto I) {
      constexpr int i = I.value;
      if constexpr (i < 64) {
        float d = dot2(kv[i][0], qv[0], 0.f);
        d = dot2(kv[i][1], qv[1], d); d = dot2(kv[i][2], qv[2], d); d = dot2(kv[i][3], qv[3], d);
        xs[i & 15] = d;
        if constexpr ((i & 15) == 15) {
#define TR_STEP(HALF, CTRL) _Pragma("unroll") for (int u = 0; u < (HALF); ++u) { \
          const float lo_ = xs[u], hi_ = xs[u + (HALF)]; const float t1_ = lo_ + dppf<CTRL>(lo_), t2_ = hi_ + dppf<CTRL>(hi_); \
          xs[u] = (c & (HALF)) ? t2_ : t1_; }
          TR_STEP(8, XOR8) TR_STEP(4, XOR7) TR_STEP(2, XOR2) TR_STEP(1, XOR1)
#undef TR_STEP
          sc[i >> 4] = xs[0] * (0.08838834764831845f * 1.4426950408889634f);
        }
        if constexpr (i == 63) {
          const float mw = wave_max(fmaxf(fmaxf(sc[0], sc[1]), fmaxf(sc[2], sc[3])));
          float e[4];
#pragma unroll
          for (int u = 0; u < 4; ++u) { e[u] = __builtin_amdgcn_exp2f(sc[u] - mw); pw[u] = cvtpk(e[u], e[u]); }
          const float lw = wave_sum((e[0] + e[1]) + (e[2] + e[3]));
          if (lane == 0) *(LAS f32x2*)&lds[L_ST + 2 * w] = f32x2{mw, lw};
          lds_arrive(lds + L_CNT + 1, lane);
          STAMP(3);
        }
      } else {
        if constexpr (i == 100) { lds_wait4(lds + L_CNT + 0); nonce = __float_as_uint(lds[L_CNT + 2]); asm volatile("" : "+v"(nonce)); }
        if constexpr (i & 1) ao = rowsmf<((i - 1) & 15)>(pw[(i - 64) >> 4], rs, kv[i - 1], kv[i], ao);
        else touch(kv[i]);
      }
      fence();
    };
    static_for<HOLD, 128 + DEPTH>([&](auto N) {
      constexpr int n = N.value;
      constexpr int c0 = (n - HOLD) * RATE < n - DEPTH ? (n - HOLD) * RATE : n - DEPTH;
      constexpr int c1 = (n + 1 - HOLD) * RATE < n + 1 - DEPTH ? (n + 1 - HOLD) * RATE : n + 1 - DEPTH;
      static_for<c0, c1>([&](auto I) { consume(I); });
      if constexpr (c1 <= n - DEPTH) { touch(kv[n - DEPTH]); fence(); }
      issue(N);
    });
    STAMP(4);
    if (r < 2) *(LAS f32x4*)((LAS char*)lds + lw) = ao;
    wg_barrier();
    if (w < NPOLL) {
      const __amdgpu_buffer_rsrc_t rw = __builtin_amdgcn_make_buffer_rsrc((void*)p.ws, 0, WS_END, RSRC_W3);
      v4u used = *(const LAS v4u*)&lds[L_USED + (lane >> 3) * 4];
      wg_barrier();
      __builtin_amdgcn_s_sleep(PD0);
      for (int i = 0; i < w; ++i) __builtin_amdgcn_s_sleep(PSTAG);
      asm volatile("" : "+v"(used));
      v4u d0, d1, d2, d3;
      unsigned long long p0 = ~0ull, p1 = ~0ull, p2 = ~0ull, p3 = ~0ull, ta, tb;
      unsigned left = SPIN_CAP, fin, c0, c1;
      asm volatile(
          "1:\n\t"
          "s_mov_b64 exec, %[p0]\n\tbuffer_load_dwordx4 %[d0], %[od], %[rw], 0 offen sc1\n\t"
          "s_mov_b64 exec, %[p1]\n\tbuffer_load_dwordx4 %[d1], %[od], %[rw], 0 offen offset:1024 sc1\n\t"
          "s_mov_b64 exec, %[p2]\n\tbuffer_load_dwordx4 %[d2], %[od], %[rw], 0 offen offset:2048 sc1\n\t"
          "s_mov_b64 exec, %[p3]\n\tbuffer_load_dwordx4 %[d3], %[od], %[rw], 0 offen offset:3072 sc1\n\t"
          "s_mov_b64 exec, -1\n\t"
          "s_sub_u32 %[left], %[left], 1\n\t"
          "s_waitcnt vmcnt(0)\n\t"
          "ds_read_b32 %[fin], %[fa]\n\t"
          WCMP(v101, v103, p0) WCMP(v105, v107, p1) WCMP(v109, v111, p2) WCMP(v113, v115, p3)
          "s_or_b64 %[ta], %[p0], %[p1]\n\ts_or_b64 %[tb], %[p2], %[p3]\n\ts_or_b64 %[ta], %[ta], %[tb]\n\t"
          "s_cbranch_scc0 2f\n\t"
          "s_waitcnt lgkmcnt(0)\n\t"
          "v_cmp_ne_u32 vcc, 0, %[fin]\n\t"
          "s_cmp_eq_u32 %[left], 0\n\t"
          "s_cbranch_scc1 2f\n\t"
          "s_cbranch_vccz 1b\n\t"
          "2:\n\t"
          "s_waitcnt lgkmcnt(0)"
          : [d0] "=&{v[100:103]}"(d0), [d1] "=&{v[104:107]}"(d1), [d2] "=&{v[108:111]}"(d2), [d3] "=&{v[112:115]}"(d3), [p0] "+s"(p0), [p1] "+s"(p1), [p2] "+s"(p2), [p3] "+s"(p3),
            [ta] "=&s"(ta), [tb] "=&s"(tb), [left] "+s"(left), [fin] "=&v"(fin), [c0] "=&s"(c0), [c1] "=&s"(c1)
          : [od] "v"(od), [rw] "s"(rw), [nn] "v"(nonce), [fa] "v"(fa), [many] "i"(MANY), [pb] "i"(PB)
          : "vcc", "scc", "memory");
      if (ta == 0ull) {
        float s0 = (__uint_as_float(d0[0]) + __uint_as_float(d1[0])) + (__uint_as_float(d2[0]) + __uint_as_float(d3[0]));
        float s1 = (__uint_as_float(d0[2]) + __uint_as_float(d1[2])) + (__uint_as_float(d2[2]) + __uint_as_float(d3[2]));
        s0 += dppf<XOR8>(s0); s1 += dppf<XOR8>(s1);
        s0 = xrow_sum(s0); s1 = xrow_sum(s1);
        if (lane < 8) __builtin_amdgcn_raw_buffer_store_b32(cvtpk(s0, s1), rout, oo, 0, AUXOUT);
        if (lane == 0) lds[L_CNT + 5] = __uint_as_float(1u);
        const v4u rep = {d0[1] & 0xffffu, d1[1] & 0xffffu, d2[1] & 0xffffu, d3[1] & 0xffffu};
        if (rep[0] != used[0] || rep[1] != used[1] || rep[2] != used[2] || rep[3] != used[3]) {
          *(LAS v4u*)&lds[L_NEW + (lane >> 3) * 4] = rep;
          lds[L_CNT + 4] = __uint_as_float(1u);
        }
        STAMPW(5);
      }
    } else {
      wg_barrier();
    }
    wg_barrier();
  } else {
    USE_ALL_ARGS();
    const int ww = w - 4, j = h >> 3;
    const int ch0 = (NCH * hp) >> 5, nch = ((NCH * (hp + 1)) >> 5) - ch0;
    const __amdgpu_buffer_rsrc_t rw = __builtin_amdgcn_make_buffer_rsrc((void*)p.ws, 0, WS_END, RSRC_W3);
    const __amdgpu_buffer_rsrc_t rq = __builtin_amdgcn_make_buffer_rsrc((void*)(p.wq), 0, DM * NH * DH * 2, RSRC_W3);
    const int vq = (128 * ww + rp) * 8192 + h * 256 + c * 16;
    const GAS unsigned short* xb = (const GAS unsigned short*)p.x + b * DM;
    __builtin_amdgcn_s_setprio(3);
    const unsigned long long t0 = __builtin_amdgcn_s_memrealtime();
    if (lane < 8 && ww == 0) lds[L_CNT + lane] = 0.f;
    const unsigned xq0 = xb[128 * ww + 4 * ((c + 4 * b) & 31) + r], xq1 = xb[128 * ww + 4 * ((16 + c + 4 * b) & 31) + r];
    v4u wq[32], w1[16], w3[16], wo[32], w2[12];
    auto ldq = [&](auto I) { wq[I.value] = __builtin_amdgcn_raw_buffer_load_b128(rq, vq, (((I.value >> 1) + 2 * b) & 15) * 65536 + (I.value & 1) * 16384, AUXW); fence(); };
    static_for<0, 4>(ldq);
    const int odn = ww == 3 ? WS_DATA + (b * 32 + hp) * 4096 + lane * 16 : OOB;
    const unsigned oldn = __builtin_amdgcn_raw_buffer_load_b32(rw, ww == 3 ? 0 : OOB, WS_R + (b * 32 + hp) * 128, AUX_SC1);
    v4u inb4[4];
#pragma unroll
    for (int q = 0; q < 4; ++q) inb4[q] = __builtin_amdgcn_raw_buffer_load_b128(rw, odn + q * 1024, 0, AUX_SC1);
    fence();
    wg_barrier();
    const __amdgpu_buffer_rsrc_t r1 = __builtin_amdgcn_make_buffer_rsrc((void*)(p.w1), 0, DM * FF * 2, RSRC_W3);
    const __amdgpu_buffer_rsrc_t r3 = __builtin_amdgcn_make_buffer_rsrc((void*)(p.w3), 0, DM * FF * 2, RSRC_W3);
    const __amdgpu_buffer_rsrc_t ro = __builtin_amdgcn_make_buffer_rsrc((void*)(p.wo + (size_t)h * DH * DM), 0, DH * DM * 2, RSRC_W3);
    const __amdgpu_buffer_rsrc_t r2 = __builtin_amdgcn_make_buffer_rsrc((void*)(p.w2 + (size_t)ch0 * 8 * DM), 0, nch * 8 * DM * 2, RSRC_W3);
    const int v13 = ((c & 7) < nch) ? (128 * ww + 2 * r + (c >> 3)) * (FF * 2) + (ch0 + (c & 7)) * 16 : OOB;
    const int vo = rp * 1024 + ww * 256 + c * 16;
    const int rot2 = b >= 6 ? b - 6 : b;
    auto ld13 = [&](auto I) {
      w1[I.value] = __builtin_amdgcn_raw_buffer_load_b128(r1, v13, ((I.value + 2 * b) & 15) * (8 * FF * 2), AUXW);
      w3[I.value] = __builtin_amdgcn_raw_buffer_load_b128(r3, v13, ((I.value + 2 * b) & 15) * (8 * FF * 2), AUXW);
      fence();
    };
    auto ldo = [&](auto I) { wo[I.value] = __builtin_amdgcn_raw_buffer_load_b128(ro, vo + (I.value & 1) * 2048, (((I.value >> 1) + 2 * b) & 15) * 8192, AUXW); fence(); };
    auto ld2 = [&](auto I) { const int s = (I.value >> 1) + rot2; w2[I.value] = __builtin_amdgcn_raw_buffer_load_b128(r2, vo + (I.value & 1) * 2048, (s >= 6 ? s - 6 : s) * 8192, AUXW); fence(); };
    v4u sel;
    {
      const unsigned hb = (c >> 3) ? 0x0302u : 0x0100u;
      const unsigned s = (c & 1) ? ((hb << 16) | 0x0c0cu) : (0x0c0c0000u | hb);
#pragma unroll
      for (int d = 0; d < 4; ++d) sel[d] = (((c & 7) >> 1) == d) ? s : 0x0c0c0c0cu;
    }
    const unsigned xq[2] = {xq0 | (xq0 << 16), xq1 | (xq1 << 16)};
    unsigned xp;
    f32x4 aq = {0.f, 0.f, 0.f, 0.f}, ag = aq, au = aq, af = aq;
    auto useq = [&](auto I) { aq = rowsmf<((2 * I.value) & 15)>(xq[I.value >> 3], rs, wq[2 * I.value], wq[2 * I.value + 1], aq); fence(); };
    auto use13 = [&](auto I) { const v4u a = pair_a<I.value>(xp, sel); ag = mfma(a, w1[I.value], ag); au = mfma(a, w3[I.value], au); fence(); };
    static_for<0, 2>(useq);
    STAMP(12);
    pace(t0, T1 + S1 * j);
    __builtin_amdgcn_s_setprio(3);
    fence();
    unsigned inb[8];
#pragma unroll
    for (int q = 0; q < 4; ++q) { touch(inb4[q]); inb[2 * q] = inb4[q][1]; inb[2 * q + 1] = inb4[q][3]; }
    fence();
    static_for<4, 32>(ldq);
    xp = *(const GAS unsigned*)(xb + 128 * ww + 8 * ((c + 2 * b) & 15) + 2 * r);
    const v4u xg = *(const GAS v4u*)(xb + 8 * lane);
    const v4u gw = *(const GAS v4u*)((const GAS unsigned short*)p.gw + 8 * lane);
    fence();
    static_for<0, 2>(ld13);
    static_for<0, 2>(ld2);
    static_for<0, 4>(ldo);
    static_for<2, 16>(useq);
    if (r < 2) *(LAS f32x4*)&lds[L_Q + ww * 128 + 8 * c + 4 * r] = aq;
    lds_arrive(lds + L_CNT + 3, lane);
    STAMP(6);
    if (ww == 3) {
      unsigned nonce = __builtin_amdgcn_readfirstlane(oldn);
      for (;;) {
        nonce = (nonce + 1u) & 0xffffu;
        bool hit = false;
#pragma unroll
        for (int q = 0; q < 4; ++q) hit |= (inb[2 * q] >> 16) == nonce || (inb[2 * q + 1] >> 16) == nonce;
        if (!__any(hit)) break;
      }
      if (lane == 0) {
        __builtin_amdgcn_raw_buffer_store_b32(nonce, rw, 0, WS_R + (b * 32 + hp) * 128, AUX_SC1);
        lds[L_CNT + 2] = __uint_as_float(nonce);
      }
    }
    static_for<0, 2>(use13);
    float g;
    {
      float d = dot2(xg[0], gw[0], 0.f);
      d = dot2(xg[1], gw[1], d); d = dot2(xg[2], gw[2], d); d = dot2(xg[3], gw[3], d);
      d = wave_sum(d);
      g = 1.f / (1.f + __expf(-d));
      asm volatile("" : "+v"(g));
    }
    pace(t0, T2 + S2 * j);
    fence();
    static_for<2, 16>(ld13);
    static_for<2, 16>(use13);
    STAMP(13);
    if ((r >> 1) == (c >> 3)) {
      *(LAS f32x4*)&lds[L_GU + ww * 256 + (r >> 1) * 128 + 8 * (c & 7) + 4 * (r & 1)] = ag;
      *(LAS f32x4*)&lds[L_GU + ww * 256 + (r >> 1) * 128 + 64 + 8 * (c & 7) + 4 * (r & 1)] = au;
    }
    asm volatile("s_waitcnt vmcnt(0)" ::: "memory");
    lds_arrive(lds + L_CNT + 0, lane);
    lds_wait4(lds + L_CNT + 0);
    STAMP(7);
    unsigned hw;
    {
      const int s2 = (c >> 1) + rot2 >= 6 ? (c >> 1) + rot2 - 6 : (c >> 1) + rot2;
      const int f = (8 * s2 + 4 * (c & 1) + r) & 63;
      float gt = 0.f, up = 0.f;
#pragma unroll
      for (int u = 0; u < 8; ++u) { gt += lds[L_GU + u * 128 + f]; up += lds[L_GU + u * 128 + 64 + f]; }
      const float hv = gt / (1.f + __expf(-gt)) * up;
      hw = (c < 12 && f < 8 * nch) ? cvtpk(hv, hv) : 0u;
    }
    pace(t0, T3 + S3 * j);
    fence();
    static_for<2, 12>(ld2);
    static_for<4, 32>(ldo);
    const int cg = 8 * ww + 2 * (c >> 2) + (r & 1);
    unsigned tag = __builtin_amdgcn_raw_buffer_load_b32(rw, WS_R + (b * 32 + cg) * 128, 0, AUX_SC1) & 0xffffu;
    fence();
    static_for<0, 6>([&](auto I) { af = rowsmf<2 * I.value>(hw, rs, w2[2 * I.value], w2[2 * I.value + 1], af); fence(); });
    af *= (1.f - g);
    touch(wo[31]);
    STAMP(14);
    lds_wait4(lds + L_CNT + 1);
    float fw[4];
    {
      f32x2 st[4];
#pragma unroll
      for (int u = 0; u < 4; ++u) st[u] = *(const LAS f32x2*)&lds[L_ST + 2 * u];
      const float M = fmaxf(fmaxf(st[0][0], st[1][0]), fmaxf(st[2][0], st[3][0]));
      float L = 0.f;
#pragma unroll
      for (int u = 0; u < 4; ++u) { fw[u] = __builtin_amdgcn_exp2f(st[u][0] - M); L += fw[u] * st[u][1]; }
      const float inv = g / L;
#pragma unroll
      for (int u = 0; u < 4; ++u) { fw[u] *= inv; asm volatile("" : "+v"(fw[u])); }
    }
    if (r < 2 && (c & 3) == 0) { lds[L_USED + cg] = __uint_as_float(tag); lds[L_NEW + cg] = __uint_as_float(tag); }
    const unsigned mine = __float_as_uint(lds[L_CNT + 2]);
    tag = (tag << 16) | mine;
    int o1_ = WS_DATA + (b * 32 + cg) * 4096 + h * 128 + (r >> 1) * 64 + (c & 3) * 16;
    int lo0_ = (L_O + 4 * ((c + 4 * b) & 31) + r) * 4, lo1_ = (L_O + 4 * ((16 + c + 4 * b) & 31) + r) * 4;
    static_for<0, 32>([&](auto I) { touch(wo[I.value]); });
    asm volatile("" : "+v"(af), "+v"(tag), "+v"(o1_), "+v"(lo0_), "+v"(lo1_));
    STAMP(8);
    wg_barrier();
    STAMP(9);
    float pa[4], pb[4];
#pragma unroll
    for (int u = 0; u < 4; ++u) { pa[u] = *(const LAS float*)((const LAS char*)lds + lo0_ + u * 512); pb[u] = *(const LAS float*)((const LAS char*)lds + lo1_ + u * 512); }
    fence();
    const float o0 = fmaf(pa[0], fw[0], pa[1] * fw[1]) + fmaf(pa[2], fw[2], pa[3] * fw[3]);
    const float o1v = fmaf(pb[0], fw[0], pb[1] * fw[1]) + fmaf(pb[2], fw[2], pb[3] * fw[3]);
    const unsigned ow[2] = {cvtpk(o0, o0), cvtpk(o1v, o1v)};
    static_for<0, 16>([&](auto I) { af = rowsmf<((2 * I.value) & 15)>(ow[I.value >> 3], rs, wo[2 * I.value], wo[2 * I.value + 1], af); });
    v4u m1 = {__float_as_uint(r < 2 ? af[0] : af[2]), tag, __float_as_uint(r < 2 ? af[1] : af[3]), tag};
    __builtin_amdgcn_raw_buffer_store_b128(m1, rw, o1_, 0, AUX_SC1);
    wg_barrier();
    STAMP(10);
    wg_barrier();
    if (__float_as_uint(lds[L_CNT + 4]) != 0u) {
      tag = (__float_as_uint(lds[L_NEW + cg]) << 16) | mine;
      m1[1] = tag; m1[3] = tag;
      __builtin_amdgcn_raw_buffer_store_b128(m1, rw, o1_, 0, AUX_SC1);
    }
  }
  if (w == 0) {
    const unsigned long long a_ = ((LAS unsigned long long*)(lds + L_STAMP))[0], b_ = ((LAS unsigned long long*)(lds + L_STAMP))[0];
    const unsigned long long now_ = __builtin_amdgcn_s_memrealtime();
    long long d_ = (long long)(b_ - a_);
    if (0) d_ = 0 - d_;
    d_ *= 0;
    for (int i_ = 0; i_ < 2000000 && (long long)(__builtin_amdgcn_s_memrealtime() - now_) < d_; ++i_) __builtin_amdgcn_s_sleep(1);
  }
}

extern "C" void kernel_launch(void* const* d_in, const int* in_sizes, int n_in, void* d_out, int out_size, void* d_ws,
                              size_t ws_size, hipStream_t stream) {
  Params p;
  p.x = (const unsigned short*)d_in[0];
  p.k = (const unsigned short*)d_in[1];
  p.v = (const unsigned short*)d_in[2];
  p.wq = (const unsigned short*)d_in[3];
  p.wo = (const unsigned short*)d_in[4];
  p.w1 = (const unsigned short*)d_in[6];
  p.w3 = (const unsigned short*)d_in[7];
  p.w2 = (const unsigned short*)d_in[8];
  p.gw = (const unsigned short*)d_in[9];
  p.out = (unsigned short*)d_out;
  p.ws = (unsigned char*)d_ws;
  hipLaunchKernelGGL(fused_kernel, dim3(NB * NH), dim3(NTHR), 0, stream, p);
}
```

```cpp
#include <hip/hip_runtime.h>
#include <hip/hip_bf16.h>

typedef unsigned v4u __attribute__((ext_vector_type(4)));
typedef unsigned v8u __attribute__((ext_vector_type(8)));
typedef float f32x4 __attribute__((ext_vector_type(4)));
typedef float f32x2 __attribute__((ext_vector_type(2)));
typedef __bf16 bf16x2 __attribute__((ext_vector_type(2)));
typedef __bf16 bf16x8 __attribute__((ext_vector_type(8)));
typedef __bf16 bf16x16 __attribute__((ext_vector_type(16)));
#define GAS __attribute__((address_space(1)))
#define LAS __attribute__((address_space(3)))
#define DEVI __device__ __forceinline__

constexpr int NB = 8, DM = 512, NH = 32, SK = 4096, DH = 128, WK = 1024, FF = 1376;
constexpr int NCH = FF / 8;
constexpr int NTHR = 512;
constexpr int RSRC_W3 = 0x00020000;
constexpr int AUX_NT = 2, AUX_SC1 = 16;
constexpr int OOB = 0x40000000;

constexpr int DEPTH = 11;
constexpr int INIT = 4;
constexpr int HOLD = 51;
constexpr int RATE = 4;
constexpr int DLOW = 4;
constexpr int DELTA = 50;
constexpr int PD0 = 16;
constexpr int T1 = 250, S1 = 88, T2 = 650, S2 = 140, T3 = 1200, S3 = 130;
constexpr int CFROM = 104;
constexpr int SCHED[6] = {1697, 1740, 1783, 1831, 1880, 1929};
constexpr int SCHED3[6] = {1563, 1671, 1780, 1830, 1880, 1929};

constexpr int WS_R = 0, WS_DATA = 256 * 128, WS_END = WS_DATA + 256 * 4096;
constexpr unsigned SPIN_CAP = 1u << 22;

constexpr int L_Q = 0;
constexpr int L_GU = L_Q + 4 * 128;
constexpr int L_O = L_GU + 4 * 256;
constexpr int L_ST = L_O + 4 * 128;
constexpr int L_USED = L_ST + 8;
constexpr int L_NEW = L_USED + 32;
constexpr int L_BM = L_NEW + 32;
constexpr int L_CNT = L_BM + 32;
constexpr int C_GU = L_CNT, C_ST = L_CNT + 1, C_Q = L_CNT + 2, F_RESEND = L_CNT + 3, W_NONCE = L_CNT + 4;
constexpr int L_END = L_CNT + 8;

template <int N> struct IC { static constexpr int value = N; };
template <int LO, int HI, class Fn> DEVI void static_for(Fn&& f) {
  if constexpr (LO < HI) { f(IC<LO>{}); static_for<LO + 1, HI>(f); }
}
DEVI float dot2(unsigned a, unsigned b, float c) {
  return __builtin_amdgcn_fdot2_f32_bf16(__builtin_bit_cast(bf16x2, a), __builtin_bit_cast(bf16x2, b), c, false);
}
DEVI float dot8(const v4u& a, const v4u& b) { return dot2(a[3], b[3], dot2(a[2], b[2], dot2(a[1], b[1], dot2(a[0], b[0], 0.f)))); }
DEVI unsigned cvtpk(float lo, float hi) {
  const bf16x2 r = {(__bf16)lo, (__bf16)hi};
  return __builtin_bit_cast(unsigned, r);
}
template <int CTRL> DEVI float dppf(float x) {
  return __builtin_bit_cast(float, __builtin_amdgcn_mov_dpp(__builtin_bit_cast(int, x), CTRL, 0xf, 0xf, true));
}
template <int CTRL> DEVI unsigned dppu(unsigned x) { return (unsigned)__builtin_amdgcn_mov_dpp((int)x, CTRL, 0xf, 0xf, true); }
constexpr int XOR1 = 0xB1, XOR2 = 0x4E, XOR7 = 0x141, XOR8 = 0x128, BCAST = 0x150;
DEVI float xrow_max(float x) {
  auto s = __builtin_amdgcn_permlane16_swap(__float_as_uint(x), __float_as_uint(x), false, false);
  x = fmaxf(__uint_as_float(s[0]), __uint_as_float(s[1]));
  auto t = __builtin_amdgcn_permlane32_swap(__float_as_uint(x), __float_as_uint(x), false, false);
  return fmaxf(__uint_as_float(t[0]), __uint_as_float(t[1]));
}
DEVI float xrow_sum(float x) {
  auto s = __builtin_amdgcn_permlane16_swap(__float_as_uint(x), __float_as_uint(x), false, false);
  x = __uint_as_float(s[0]) + __uint_as_float(s[1]);
  auto t = __builtin_amdgcn_permlane32_swap(__float_as_uint(x), __float_as_uint(x), false, false);
  return __uint_as_float(t[0]) + __uint_as_float(t[1]);
}
DEVI float wave_max(float x) {
  x = fmaxf(x, dppf<XOR1>(x)); x = fmaxf(x, dppf<XOR2>(x)); x = fmaxf(x, dppf<XOR7>(x)); x = fmaxf(x, dppf<XOR8>(x));
  return xrow_max(x);
}
DEVI float wave_sum(float x) {
  x += dppf<XOR1>(x); x += dppf<XOR2>(x); x += dppf<XOR7>(x); x += dppf<XOR8>(x);
  return xrow_sum(x);
}

struct RowSel { unsigned m0, m1; int idx; };
template <int T> DEVI v4u row_a(unsigned word, const RowSel& q) {
  return v4u{dppu<BCAST + T>(word) & q.m0, dppu<BCAST + T>(word) & q.m1, dppu<BCAST + T + 1>(word) & q.m0, dppu<BCAST + T + 1>(word) & q.m1};
}
DEVI f32x4 smf(const v4u& a, const RowSel& q, const v4u& b0, const v4u& b1, f32x4 acc) {
  const v8u b = __builtin_shufflevector(b0, b1, 0, 1, 2, 3, 4, 5, 6, 7);
  return __builtin_amdgcn_smfmac_f32_16x16x64_bf16(__builtin_bit_cast(bf16x8, a), __builtin_bit_cast(bf16x16, b), acc, q.idx, 0, 0);
}
template <int T> DEVI v4u pair_a(unsigned word, const v4u& sel) {
  const unsigned wb = dppu<BCAST + T>(word);
  return v4u{__builtin_amdgcn_perm(wb, wb, sel[0]), __builtin_amdgcn_perm(wb, wb, sel[1]),
             __builtin_amdgcn_perm(wb, wb, sel[2]), __builtin_amdgcn_perm(wb, wb, sel[3])};
}
DEVI f32x4 mfma(const v4u& a, const v4u& b, f32x4 acc) {
  return __builtin_amdgcn_mfma_f32_16x16x32_bf16(__builtin_bit_cast(bf16x8, a), __builtin_bit_cast(bf16x8, b), acc, 0, 0, 0);
}

DEVI __amdgpu_buffer_rsrc_t rsrc(const void* p, int bytes) { return __builtin_amdgcn_make_buffer_rsrc((void*)p, 0, bytes, RSRC_W3); }
DEVI void touch(const v4u& x) { asm volatile("" :: "v"(x)); }
DEVI void fence() { __builtin_amdgcn_sched_barrier(0); }
DEVI void wg_barrier() { asm volatile("s_waitcnt lgkmcnt(0)\n\ts_barrier" ::: "memory"); }
DEVI void pace(unsigned long long t0, int ticks) {
  for (int i = 0; i < 4096 && (int)(__builtin_amdgcn_s_memrealtime() - t0) < ticks; ++i) __builtin_amdgcn_s_sleep(4);
}
DEVI void lds_arrive(LAS float* p, int lane) {
  asm volatile("" ::: "memory");
  if (lane == 0) __hip_atomic_fetch_add((LAS unsigned*)p, 1u, __ATOMIC_RELAXED, __HIP_MEMORY_SCOPE_WORKGROUP);
}
DEVI void lds_wait4(LAS float* p) {
  while (__hip_atomic_load((LAS unsigned*)p, __ATOMIC_RELAXED, __HIP_MEMORY_SCOPE_WORKGROUP) < 4u) __builtin_amdgcn_s_sleep(1);
  asm volatile("" ::: "memory");
}

struct Params {
  const unsigned short *k, *wq, *x, *v, *wo, *w1, *w3;
  unsigned char* ws;
  const unsigned short *w2, *gw;
  unsigned short* out;
};
struct LateArgs { const unsigned short *w2, *gw; unsigned short* out; };
DEVI LateArgs late_args() {
  typedef const __attribute__((address_space(4))) unsigned long long* KA;
  KA ka = (KA)__builtin_amdgcn_kernarg_segment_ptr();
  asm volatile("" : "+s"(ka));
  return LateArgs{(const unsigned short*)ka[8], (const unsigned short*)ka[9], (unsigned short*)ka[10]};
}
static_assert(sizeof(Params) == 88, "late_args() reads Params by offset");

__global__ void __launch_bounds__(NTHR, 2) __attribute__((amdgpu_waves_per_eu(2, 2))) fused_kernel(Params p) {
  __shared__ __attribute__((aligned(16))) float lds_[L_END];
  LAS float* lds = (LAS float*)lds_;
  const int tid = threadIdx.x, lane = tid & 63, w = __builtin_amdgcn_readfirstlane(tid >> 6);
  const int r = lane >> 4, c = lane & 15;
  const int bid = blockIdx.x, b = bid >> 5, j = (bid >> 3) & 3;
  const int h = (bid & 7) * 4 + j;
  RowSel rs;
  {
    const unsigned hm = (c & 3) == 3 ? 0xffff0000u : 0x0000ffffu;
    rs.m0 = (c & 4) ? 0u : hm; rs.m1 = (c & 4) ? hm : 0u;
    rs.idx = (((c & 3) == 3 ? 0 : (c & 3)) | 12) * 0x1111;
  }
  const int rp = (r >> 1) + 4 * (r & 1);
#define USE_FIRST_ARGS() asm volatile("" :: "s"(p.k), "s"(p.wq), "s"(p.x), "s"(p.v), "s"(p.wo), "s"(p.w1), "s"(p.w3), "s"(p.ws))

  if (w >= 4) {
    USE_FIRST_ARGS();
    __builtin_amdgcn_s_setprio(3);
    const unsigned long long t0 = __builtin_amdgcn_s_memrealtime();
    const int ww = w - 4;
    const int ch0 = (NCH * h) >> 5, nch = ((NCH * (h + 1)) >> 5) - ch0;
    if (lane < 8 && ww == 0) lds[L_CNT + lane] = 0.f;
    const __amdgpu_buffer_rsrc_t rq = rsrc(p.wq, DM * NH * DH * 2);
    const GAS unsigned short* xb = (const GAS unsigned short*)p.x + b * DM;
    const int vq = (128 * ww + rp) * 8192 + h * 256 + c * 16;
    const unsigned xq0 = xb[128 * ww + 4 * ((c + 4 * b) & 31) + r], xq1 = xb[128 * ww + 4 * ((16 + c + 4 * b) & 31) + r];
    v4u wq[32], w1[16], w3[16], wo[32], w2[12];
    auto ldq = [&](auto I) { wq[I.value] = __builtin_amdgcn_raw_buffer_load_b128(rq, vq, (((I.value >> 1) + 2 * b) & 15) * 65536 + (I.value & 1) * 16384, 0); fence(); };
    static_for<0, 4>(ldq);
    fence();
    wg_barrier();
    const __amdgpu_buffer_rsrc_t rw = rsrc(p.ws, WS_END);
    const unsigned oldn = __builtin_amdgcn_raw_buffer_load_b32(rw, ww == 3 ? 0 : OOB, WS_R + (b * 32 + h) * 128, AUX_SC1);
    v4u inb4[4];
#pragma unroll
    for (int q = 0; q < 4; ++q)
      inb4[q] = __builtin_amdgcn_raw_buffer_load_b128(rw, (ww == 3 ? WS_DATA + (b * 32 + h) * 4096 + lane * 16 : OOB) + q * 1024, 0, AUX_SC1);
    fence();
    const LateArgs a1 = late_args();
    const __amdgpu_buffer_rsrc_t r1 = rsrc(p.w1, DM * FF * 2), r3 = rsrc(p.w3, DM * FF * 2);
    const __amdgpu_buffer_rsrc_t ro = rsrc(p.wo + (size_t)h * DH * DM, DH * DM * 2), r2 = rsrc(a1.w2 + (size_t)ch0 * 8 * DM, nch * 8 * DM * 2);
    const int v13 = ((c & 7) < nch) ? (128 * ww + 2 * r + (c >> 3)) * (FF * 2) + (ch0 + (c & 7)) * 16 : OOB;
    const int vo = rp * 1024 + ww * 256 + c * 16;
    const int rot2 = b >= 6 ? b - 6 : b;
    auto ld13 = [&](auto I) {
      w1[I.value] = __builtin_amdgcn_raw_buffer_load_b128(r1, v13, ((I.value + 2 * b) & 15) * (8 * FF * 2), 0);
      w3[I.value] = __builtin_amdgcn_raw_buffer_load_b128(r3, v13, ((I.value + 2 * b) & 15) * (8 * FF * 2), 0);
      fence();
    };
    auto ldo = [&](auto I) { wo[I.value] = __builtin_amdgcn_raw_buffer_load_b128(ro, vo + (I.value & 1) * 2048, (((I.value >> 1) + 2 * b) & 15) * 8192, 0); fence(); };
    auto ld2 = [&](auto I) {
      const int s = (I.value >> 1) + rot2;
      w2[I.value] = __builtin_amdgcn_raw_buffer_load_b128(r2, vo + (I.value & 1) * 2048, (s >= 6 ? s - 6 : s) * 8192, 0);
      fence();
    };
    v4u sel;
    {
      const unsigned hb = (c >> 3) ? 0x0302u : 0x0100u, s = (c & 1) ? ((hb << 16) | 0x0c0cu) : (0x0c0c0000u | hb);
#pragma unroll
      for (int d = 0; d < 4; ++d) sel[d] = (((c & 7) >> 1) == d) ? s : 0x0c0c0c0cu;
    }
    const unsigned xq[2] = {xq0 | (xq0 << 16), xq1 | (xq1 << 16)};
    unsigned xp;
    f32x4 aq = {0.f, 0.f, 0.f, 0.f}, ag = aq, au = aq, af = aq;
    auto useq = [&](auto I) { aq = smf(row_a<((2 * I.value) & 15)>(xq[I.value >> 3], rs), rs, wq[2 * I.value], wq[2 * I.value + 1], aq); fence(); };
    auto use13 = [&](auto I) { const v4u a = pair_a<I.value>(xp, sel); ag = mfma(a, w1[I.value], ag); au = mfma(a, w3[I.value], au); fence(); };

    static_for<0, 2>(useq);
    pace(t0, T1 + S1 * j);
    unsigned inb[8];
#pragma unroll
    for (int q = 0; q < 4; ++q) { touch(inb4[q]); inb[2 * q] = inb4[q][1] >> 16; inb[2 * q + 1] = inb4[q][3] >> 16; }
    fence();
    static_for<4, 32>(ldq);
    xp = *(const GAS unsigned*)(xb + 128 * ww + 8 * ((c + 2 * b) & 15) + 2 * r);
    const v4u xg = *(const GAS v4u*)(xb + 8 * lane), gw = *(const GAS v4u*)((const GAS unsigned short*)a1.gw + 8 * lane);
    fence();
    static_for<0, 2>(ld13);
    static_for<0, 2>(ld2);
    static_for<0, 4>(ldo);
    static_for<2, 16>(useq);
    if (r < 2) *(LAS f32x4*)&lds[L_Q + ww * 128 + 8 * c + 4 * r] = aq;
    lds_arrive(lds + C_Q, lane);
    if (ww == 3) {
      const unsigned first = __builtin_amdgcn_readfirstlane(oldn) + 1u;
      LAS unsigned* bm = (LAS unsigned*)(lds + L_BM);
      if (lane < 32) bm[lane] = 0u;
      asm volatile("" ::: "memory");
#pragma unroll
      for (int q = 0; q < 8; ++q) {
        const unsigned d = (inb[q] - first) & 0xffffu;
        __hip_atomic_fetch_or(bm + ((d >> 5) & 31u), d < 1024u ? 1u << (d & 31u) : 0u, __ATOMIC_RELAXED, __HIP_MEMORY_SCOPE_WORKGROUP);
      }
      asm volatile("" ::: "memory");
      const unsigned fr = ~bm[lane & 31];
      const int f = __builtin_ctzll(__ballot(fr != 0u));
      const unsigned nonce = (first + 32u * f + __builtin_ctz(__builtin_amdgcn_readlane(fr, f))) & 0xffffu;
      if (lane == 0) {
        __builtin_amdgcn_raw_buffer_store_b32(nonce, rw, 0, WS_R + (b * 32 + h) * 128, AUX_SC1);
        lds[W_NONCE] = __uint_as_float(nonce);
      }
    }
    static_for<0, 2>(use13);
    float g = 1.f / (1.f + __expf(-wave_sum(dot8(xg, gw))));
    asm volatile("" : "+v"(g));

    pace(t0, T2 + S2 * j);
    fence();
    static_for<2, 16>(ld13);
    static_for<2, 16>(use13);
    if ((r >> 1) == (c >> 3)) {
      *(LAS f32x4*)&lds[L_GU + ww * 256 + (r >> 1) * 128 + 8 * (c & 7) + 4 * (r & 1)] = ag;
      *(LAS f32x4*)&lds[L_GU + ww * 256 + (r >> 1) * 128 + 64 + 8 * (c & 7) + 4 * (r & 1)] = au;
    }
    asm volatile("s_waitcnt vmcnt(0)" ::: "memory");
    lds_arrive(lds + C_GU, lane);
    lds_wait4(lds + C_GU);
    unsigned hw;
    {
      const int s2 = (c >> 1) + rot2 >= 6 ? (c >> 1) + rot2 - 6 : (c >> 1) + rot2;
      const int f = (8 * s2 + 4 * (c & 1) + r) & 63;
      float gt = 0.f, up = 0.f;
#pragma unroll
      for (int u = 0; u < 8; ++u) { gt += lds[L_GU + u * 128 + f]; up += lds[L_GU + u * 128 + 64 + f]; }
      const float hv = gt / (1.f + __expf(-gt)) * up;
      hw = (c < 12 && f < 8 * nch) ? cvtpk(hv, hv) : 0u;
    }

    pace(t0, T3 + S3 * j);
    fence();
    const int cg = 8 * ww + 2 * (c >> 2) + (r & 1);
    unsigned tag = __builtin_amdgcn_raw_buffer_load_b32(rw, WS_R + (b * 32 + cg) * 128, 0, AUX_SC1);
    fence();
    static_for<2, 12>(ld2);
    static_for<4, 32>(ldo);
    static_for<0, 6>([&](auto I) { af = smf(row_a<2 * I.value>(hw, rs), rs, w2[2 * I.value], w2[2 * I.value + 1], af); fence(); });
    af *= (1.f - g);
    lds_wait4(lds + C_ST);
    float fw[4];
    {
      f32x2 st[4];
#pragma unroll
      for (int u = 0; u < 4; ++u) st[u] = *(const LAS f32x2*)&lds[L_ST + 2 * u];
      const float M = fmaxf(fmaxf(st[0][0], st[1][0]), fmaxf(st[2][0], st[3][0]));
      float L = 0.f;
#pragma unroll
      for (int u = 0; u < 4; ++u) { fw[u] = __builtin_amdgcn_exp2f(st[u][0] - M); L += fw[u] * st[u][1]; }
      const float inv = g / L;
#pragma unroll
      for (int u = 0; u < 4; ++u) { fw[u] *= inv; asm volatile("" : "+v"(fw[u])); }
    }
    tag &= 0xffffu;
    if (r < 2 && (c & 3) == 0) { lds[L_USED + cg] = __uint_as_float(tag); lds[L_NEW + cg] = __uint_as_float(tag); }
    const unsigned mine = __float_as_uint(lds[W_NONCE]);
    tag = (tag << 16) | mine;
    int om = WS_DATA + (b * 32 + cg) * 4096 + (bid & 31) * 128 + (r >> 1) * 64 + (c & 3) * 16;
    int lo0 = (L_O + 4 * ((c + 4 * b) & 31) + r) * 4, lo1 = (L_O + 4 * ((16 + c + 4 * b) & 31) + r) * 4;
    static_for<0, 32>([&](auto I) { touch(wo[I.value]); });
    asm volatile("" : "+v"(af), "+v"(tag), "+v"(om), "+v"(lo0), "+v"(lo1));

    wg_barrier();
    float pa[4], pb[4];
#pragma unroll
    for (int u = 0; u < 4; ++u) pa[u] = *(const LAS float*)((const LAS char*)lds + lo0 + u * 512);
#pragma unroll
    for (int u = 0; u < 4; ++u) pb[u] = *(const LAS float*)((const LAS char*)lds + lo1 + u * 512);
    fence();
    auto merge = [&](const float (&x)[4]) { const float o = fmaf(x[3], fw[3], fmaf(x[2], fw[2], fmaf(x[1], fw[1], x[0] * fw[0]))); return cvtpk(o, o); };
    unsigned ow[2];
    v4u a[2];
    ow[0] = merge(pa);
    a[0] = row_a<0>(ow[0], rs);
    fence();
    static_for<0, 16>([&](auto I) {
      constexpr int i = I.value;
      if constexpr (i + 1 < 16) a[(i + 1) & 1] = row_a<((2 * i + 2) & 15)>(ow[(i + 1) >> 3], rs);
      fence();
      af = smf(a[i & 1], rs, wo[2 * i], wo[2 * i + 1], af);
      if constexpr (i == 3) ow[1] = merge(pb);
      fence();
    });
    v4u m = {__float_as_uint(r < 2 ? af[0] : af[2]), tag, __float_as_uint(r < 2 ? af[1] : af[3]), tag};
    __builtin_amdgcn_raw_buffer_store_b128(m, rw, om, 0, AUX_SC1);
    wg_barrier();
    wg_barrier();
    if (__float_as_uint(lds[F_RESEND]) != 0u) {
      m[1] = m[3] = (__float_as_uint(lds[L_NEW + cg]) << 16) | mine;
      __builtin_amdgcn_raw_buffer_store_b128(m, rw, om, 0, AUX_SC1);
    }
  } else {
    USE_FIRST_ARGS();
    const size_t kvo = ((size_t)(b * NH + h) * SK + (SK - WK)) * DH;
    const __amdgpu_buffer_rsrc_t rk = rsrc(p.k + kvo, WK * DH * 2), rv = rsrc(p.v + kvo, WK * DH * 2);
    const int vk = w * 4096 + lane * 16, vv = w * 4096 + rp * 256 + c * 16;
    v4u kv[128];
    const unsigned ts0 = (unsigned)__builtin_amdgcn_s_memrealtime();
    unsigned tnow = 0, tel = 1, tth = 0;
    auto issue = [&](auto N) {
      constexpr int n = N.value;
      if constexpr (n >= CFROM && n < 128) {
        if constexpr ((n & 3) == 0) tnow = (unsigned)__builtin_amdgcn_s_memrealtime();
        if constexpr ((n & 3) == 1) { tel = tnow - ts0; tth = (j == 3 ? SCHED3[(n - CFROM) / 4] : SCHED[(n - CFROM) / 4]) + DELTA; }
        if constexpr (n > CFROM)
          asm volatile("s_cmp_lt_u32 %0, %1\n\ts_cbranch_scc0 1f\n\ts_waitcnt vmcnt(%2)\n1:" :: "s"(tel), "s"(tth), "i"(DLOW - 1) : "scc");
        fence();
      }
      if constexpr (n < 64) kv[n] = __builtin_amdgcn_raw_buffer_load_b128(rk, vk + (n & 3) * 1024, (n >> 2) * 16384, AUX_NT);
      else if constexpr (n < 128) kv[n] = __builtin_amdgcn_raw_buffer_load_b128(rv, vv + (n & 1) * 512 + (n & 2) * 1024, ((n >> 2) & 15) * 16384, AUX_NT);
      fence();
    };
    static_for<0, INIT>([&](auto N) { issue(N); });
    asm volatile("s_barrier" ::: "memory");
    static_for<INIT, DEPTH>([&](auto N) { issue(N); });
    static_for<DEPTH, HOLD>([&](auto N) { touch(kv[N.value - DEPTH]); fence(); issue(N); });
    lds_wait4(lds + C_Q);
    v4u qv;
    {
      f32x4 q0 = *(const LAS f32x4*)&lds[L_Q + 8 * c], q1 = *(const LAS f32x4*)&lds[L_Q + 8 * c + 4];
#pragma unroll
      for (int u = 1; u < 4; ++u) { q0 += *(const LAS f32x4*)&lds[L_Q + u * 128 + 8 * c]; q1 += *(const LAS f32x4*)&lds[L_Q + u * 128 + 8 * c + 4]; }
      qv = v4u{cvtpk(q0[0], q0[1]), cvtpk(q0[2], q0[3]), cvtpk(q1[0], q1[1]), cvtpk(q1[2], q1[3])};
    }
    const LateArgs a1 = late_args();
    int od = WS_DATA + (b * 32 + h) * 4096 + lane * 16, lw = (L_O + w * 128 + 8 * c + 4 * (r & 1)) * 4;
    int oo = (b * DM + 32 * (h >> 1) + 4 * (h & 1) + 8 * (lane & 3) + 2 * ((lane >> 2) & 1)) * 2;
    asm volatile("" : "+v"(od), "+v"(lw), "+v"(oo));
    float xs[16], sc[4];
    unsigned pw[4];
    f32x4 ao = {0.f, 0.f, 0.f, 0.f};
    auto consume = [&](auto I) {
      constexpr int i = I.value;
      if constexpr (i < 64) {
        xs[i & 15] = dot8(kv[i], qv);
        if constexpr ((i & 15) == 15) {
#define TR_STEP(HALF, CTRL) _Pragma("unroll") for (int u = 0; u < (HALF); ++u) { \
          const float lo_ = xs[u], hi_ = xs[u + (HALF)]; const float t1_ = lo_ + dppf<CTRL>(lo_), t2_ = hi_ + dppf<CTRL>(hi_); \
          xs[u] = (c & (HALF)) ? t2_ : t1_; }
          TR_STEP(8, XOR8) TR_STEP(4, XOR7) TR_STEP(2, XOR2) TR_STEP(1, XOR1)
#undef TR_STEP
          sc[i >> 4] = xs[0] * (0.08838834764831845f * 1.4426950408889634f);
        }
        if constexpr (i == 63) {
          const float mw = wave_max(fmaxf(fmaxf(sc[0], sc[1]), fmaxf(sc[2], sc[3])));
          float e[4];
#pragma unroll
          for (int u = 0; u < 4; ++u) { e[u] = __builtin_amdgcn_exp2f(sc[u] - mw); pw[u] = cvtpk(e[u], e[u]); }
          const float sw = wave_sum((e[0] + e[1]) + (e[2] + e[3]));
          if (lane == 0) *(LAS f32x2*)&lds[L_ST + 2 * w] = f32x2{mw, sw};
          lds_arrive(lds + C_ST, lane);
        }
      } else if constexpr (i & 1) {
        ao = smf(row_a<((i - 1) & 15)>(pw[(i - 64) >> 4], rs), rs, kv[i - 1], kv[i], ao);
      } else {
        touch(kv[i]);
      }
      fence();
    };
    static_for<HOLD, 128 + DEPTH>([&](auto N) {
      constexpr int n = N.value;
      constexpr int c0 = (n - HOLD) * RATE < n - DEPTH ? (n - HOLD) * RATE : n - DEPTH;
      constexpr int c1 = (n + 1 - HOLD) * RATE < n + 1 - DEPTH ? (n + 1 - HOLD) * RATE : n + 1 - DEPTH;
      static_for<c0, c1>([&](auto I) { consume(I); });
      if constexpr (c1 <= n - DEPTH) { touch(kv[n - DEPTH]); fence(); }
      issue(N);
    });
    *(LAS f32x4*)((LAS char*)lds + lw) = ao;
    wg_barrier();
    if (w == 0) {
      const __amdgpu_buffer_rsrc_t rw = rsrc(p.ws, WS_END), ro = rsrc(a1.out, NB * DM * 2);
      v4u used = *(const LAS v4u*)&lds[L_USED + (lane >> 3) * 4];
      unsigned nonce = __float_as_uint(lds[W_NONCE]);
      wg_barrier();
      asm volatile("" : "+v"(used), "+v"(nonce));
      __builtin_amdgcn_s_sleep(PD0);
      v4u d0, d1, d2, d3;
      unsigned long long p0 = ~0ull, p1 = ~0ull, p2 = ~0ull, p3 = ~0ull, ta, tb;
      unsigned left = SPIN_CAP;
#define POLL_LD(p, d, off, l) "s_mov_b64 exec, %[" #p "]\n\ts_cbranch_execz " #l "f\n\tbuffer_load_dwordx4 %[" #d "], %[od], %[rw], 0 offen offset:" #off " sc1\n" #l ":\n\t"
#define POLL_CMP(a, b, p) "v_cmp_ne_u32_sdwa %[ta], " #a ", %[nn] src0_sel:WORD_1 src1_sel:DWORD\n\t" \
                          "v_cmp_ne_u32_sdwa %[tb], " #b ", %[nn] src0_sel:WORD_1 src1_sel:DWORD\n\ts_or_b64 %[" #p "], %[ta], %[tb]\n\t"
      asm volatile(
          "1:\n\t"
          POLL_LD(p0, d0, 0, 3) POLL_LD(p1, d1, 1024, 4) POLL_LD(p2, d2, 2048, 5) POLL_LD(p3, d3, 3072, 6)
          "s_mov_b64 exec, -1\n\t"
          "s_waitcnt vmcnt(0)\n\t"
          POLL_CMP(v101, v103, p0) POLL_CMP(v105, v107, p1) POLL_CMP(v109, v111, p2) POLL_CMP(v113, v115, p3)
          "s_or_b64 %[ta], %[p0], %[p1]\n\ts_or_b64 %[tb], %[p2], %[p3]\n\ts_or_b64 %[ta], %[ta], %[tb]\n\t"
          "s_cbranch_scc0 2f\n\t"
          "s_sub_u32 %[left], %[left], 1\n\t"
          "s_cbranch_scc0 1b\n"
          "2:"
          : [d0] "=&{v[100:103]}"(d0), [d1] "=&{v[104:107]}"(d1), [d2] "=&{v[108:111]}"(d2), [d3] "=&{v[112:115]}"(d3),
            [p0] "+s"(p0), [p1] "+s"(p1), [p2] "+s"(p2), [p3] "+s"(p3), [ta] "=&s"(ta), [tb] "=&s"(tb), [left] "+s"(left)
          : [od] "v"(od), [rw] "s"(rw), [nn] "v"(nonce)
          : "scc", "memory");
      if (ta == 0ull) {
        float s0 = (__uint_as_float(d0[0]) + __uint_as_float(d1[0])) + (__uint_as_float(d2[0]) + __uint_as_float(d3[0]));
        float s1 = (__uint_as_float(d0[2]) + __uint_as_float(d1[2])) + (__uint_as_float(d2[2]) + __uint_as_float(d3[2]));
        s0 += dppf<XOR8>(s0); s1 += dppf<XOR8>(s1);
        s0 = xrow_sum(s0); s1 = xrow_sum(s1);
        if (lane < 8) __builtin_amdgcn_raw_buffer_store_b32(cvtpk(s0, s1), ro, oo, 0, 0);
        const v4u rep = {d0[1] & 0xffffu, d1[1] & 0xffffu, d2[1] & 0xffffu, d3[1] & 0xffffu};
        if (rep[0] != used[0] || rep[1] != used[1] || rep[2] != used[2] || rep[3] != used[3]) {
          *(LAS v4u*)&lds[L_NEW + (lane >> 3) * 4] = rep;
          lds[F_RESEND] = __uint_as_float(1u);
        }
      }
    } else {
      wg_barrier();
    }
    wg_barrier();
  }
}

extern "C" void kernel_launch(void* const* d_in, const int* in_sizes, int n_in, void* d_out, int out_size, void* d_ws,
                              size_t ws_size, hipStream_t stream) {
  Params p;
  p.x = (const unsigned short*)d_in[0];
  p.k = (const unsigned short*)d_in[1];
  p.v = (const unsigned short*)d_in[2];
  p.wq = (const unsigned short*)d_in[3];
  p.wo = (const unsigned short*)d_in[4];
  p.w1 = (const unsigned short*)d_in[6];
  p.w3 = (const unsigned short*)d_in[7];
  p.w2 = (const unsigned short*)d_in[8];
  p.gw = (const unsigned short*)d_in[9];
  p.out = (unsigned short*)d_out;
  p.ws = (unsigned char*)d_ws;
  hipLaunchKernelGGL(fused_kernel, dim3(NB * NH), dim3(NTHR), 0, stream, p);
}
```
